```python
import torch

def _softmax(x, axis=-1):
    return torch.softmax(x, dim=axis)
def _gelu(x):
    return torch.nn.functional.gelu(x, approximate="tanh")
def _silu(x):
    return torch.nn.functional.silu(x)
def _sigmoid(x):
    return torch.sigmoid(x)
def _layernorm(x, axis=-1, eps=1e-5):
    m = x.mean(dim=axis, keepdim=True)
    v = x.var(dim=axis, keepdim=True, unbiased=False)
    return (x - m) / torch.sqrt(v + eps)
def _scatter_add(tgt, idx, src, axis=0):
    return tgt.clone().index_add_(axis, idx, src)
def _segment_sum(src, seg, n):
    out = torch.zeros((n,) + src.shape[1:], dtype=src.dtype)
    return out.index_add_(0, seg, src)
def _pad(x, pw):
    flat = tuple(v for pair in reversed(pw) for v in pair)
    return torch.nn.functional.pad(x, flat)

kernel_name = 'standalone__qk_norm__prompt'

def setup_inputs():

    _w = (lambda a: (lambda t: t.to(torch.bfloat16) if t.is_floating_point() else t)(torch.as_tensor(a)))
    B, H, S, D = 4, 16, 2048, 448
    Dh = D // H
    return {
        "q": _w((lambda *s: torch.randn(*s, dtype=torch.bfloat16) * 0.05)(B, H, S, Dh)),
        "k": _w((lambda *s: torch.randn(*s, dtype=torch.bfloat16) * 0.05)(B, H, S, Dh)),
        "v": _w((lambda *s: torch.randn(*s, dtype=torch.bfloat16) * 0.8)(B, H, S, Dh)),
        "t": _w(torch.abs((lambda *s: torch.randn(*s, dtype=torch.bfloat16) * 0.8)(H)) + 1.0),
    }

def reference(q, k, v, t):
    eps = 1e-6
    qn = q / (torch.sqrt((q * q).sum(axis=-1, keepdims=True)) + eps)
    kn = k / (torch.sqrt((k * k).sum(axis=-1, keepdims=True)) + eps)
    logits = torch.einsum('bhqd,bhkd->bhqk', qn, kn) * t[None, :, None, None]
    attn = _softmax(logits, axis=-1)
    return torch.einsum('bhqk,bhkd->bhqd', attn, v)
```

```cpp
#include <hip/hip_runtime.h>
#include <hip/hip_bf16.h>
#include <stdint.h>

typedef _Float16 f16x8 __attribute__((ext_vector_type(8)));
typedef _Float16 f16x2 __attribute__((ext_vector_type(2)));
typedef __bf16 bf16x2 __attribute__((ext_vector_type(2)));
typedef __bf16 bf16x8 __attribute__((ext_vector_type(8)));
typedef __bf16 bf16x16 __attribute__((ext_vector_type(16)));
typedef float f32x2 __attribute__((ext_vector_type(2)));
typedef float f32x8 __attribute__((ext_vector_type(8)));
typedef float f32x16 __attribute__((ext_vector_type(16)));
typedef unsigned u32x2 __attribute__((ext_vector_type(2)));
typedef unsigned u32x3 __attribute__((ext_vector_type(3)));
typedef unsigned u32x4 __attribute__((ext_vector_type(4)));
typedef short s16x4 __attribute__((ext_vector_type(4)));
typedef __attribute__((address_space(3))) char* lds_ptr;

#define DI __device__ __forceinline__
#define SBAR() __builtin_amdgcn_sched_barrier(0)

constexpr int SEQ = 2048, DH = 28, ROWB = 56, NHEADS = 16, NBH = 64;
constexpr int NW = 8;
constexpr int CHUNK = 256, NCH = SEQ / CHUNK, TPC = CHUNK / 32;
constexpr int KSLOT = TPC * 2048;
constexpr int VSLOT = CHUNK * ROWB;
constexpr int LDS_K = 0, LDS_V = 2 * KSLOT, LDS_ONES = LDS_V + 2 * VSLOT, ONES_B = 14336;
constexpr int LDS_OST = LDS_ONES + ONES_B;
constexpr int LDS_BYTES = LDS_OST + NW * 3584;
constexpr float T_FAST = 10.0f;
constexpr float LOG2E = 1.4426950408889634f;
constexpr int SP_IDX = (int)0xDDDDDDDD;

struct Raw7 { u32x4 a; u32x3 b; };

DI Raw7 load_raw7(const char* p) {
    Raw7 r;
    r.a = *reinterpret_cast<const __attribute__((aligned(4))) u32x4*>(p);
    r.b = *reinterpret_cast<const __attribute__((aligned(4))) u32x3*>(p + 16);
    return r;
}
DI float bf_lo(unsigned w) { return __uint_as_float(w << 16); }
DI float bf_hi(unsigned w) { return __uint_as_float(w & 0xffff0000u); }
DI unsigned pk_f16(float a, float b) { f32x2 v = {a, b}; return __builtin_bit_cast(unsigned, __builtin_convertvector(v, f16x2)); }
DI unsigned pk_bf16(float a, float b) { f32x2 v = {a, b}; return __builtin_bit_cast(unsigned, __builtin_convertvector(v, bf16x2)); }
DI float dot2bf(unsigned a, float c) { return __builtin_amdgcn_fdot2_f32_bf16(__builtin_bit_cast(bf16x2, a), __builtin_bit_cast(bf16x2, a), c, false); }

DI float sumsq7(const Raw7& r) {
    float s0 = dot2bf(r.a[0], 0.f), s1 = dot2bf(r.a[1], 0.f);
    s0 = dot2bf(r.a[2], s0); s1 = dot2bf(r.a[3], s1);
    s0 = dot2bf(r.b[0], s0); s1 = dot2bf(r.b[1], s1);
    s0 = dot2bf(r.b[2], s0);
    return s0 + s1;
}
DI void scale7(const Raw7& r, float sc, unsigned pad, u32x4& f0, u32x4& f1) {
    f0[0] = pk_f16(bf_lo(r.a[0]) * sc, bf_hi(r.a[0]) * sc);
    f0[1] = pk_f16(bf_lo(r.a[1]) * sc, bf_hi(r.a[1]) * sc);
    f0[2] = pk_f16(bf_lo(r.a[2]) * sc, bf_hi(r.a[2]) * sc);
    f0[3] = pk_f16(bf_lo(r.a[3]) * sc, bf_hi(r.a[3]) * sc);
    f1[0] = pk_f16(bf_lo(r.b[0]) * sc, bf_hi(r.b[0]) * sc);
    f1[1] = pk_f16(bf_lo(r.b[1]) * sc, bf_hi(r.b[1]) * sc);
    f1[2] = pk_f16(bf_lo(r.b[2]) * sc, bf_hi(r.b[2]) * sc);
    f1[3] = pad;
}
DI float inv_norm(float ss) { return __builtin_amdgcn_rcpf(__builtin_amdgcn_sqrtf(ss) + 1e-6f); }
DI float xhalf_sum(float x) {
    auto rr = __builtin_amdgcn_permlane32_swap(__float_as_uint(x), __float_as_uint(x), false, false);
    return __uint_as_float(rr[0]) + __uint_as_float(rr[1]);
}
DI float xhalf_max(float x) {
    auto rr = __builtin_amdgcn_permlane32_swap(__float_as_uint(x), __float_as_uint(x), false, false);
    return __builtin_fmaxf(__uint_as_float(rr[0]), __uint_as_float(rr[1]));
}
DI void glds16(const void* g, unsigned lds_base) {
    unsigned sv;
    asm volatile("s_mov_b32 %0, m0\n\ts_mov_b32 m0, %2\n\ts_nop 0\n\tglobal_load_lds_dwordx4 %1, off\n\ts_mov_b32 m0, %0"
                 : "=&s"(sv) : "v"(g), "s"(lds_base) : "memory");
}
#define WAIT_BAR() asm volatile("s_waitcnt vmcnt(0) lgkmcnt(0)\n\ts_barrier" ::: "memory")

DI f16x8 lds_rd128(lds_ptr p) { return *reinterpret_cast<const __attribute__((address_space(3))) f16x8*>(p); }
DI s16x4 lds_rdtr(lds_ptr p) { return __builtin_amdgcn_ds_read_tr16_b64_v4i16(reinterpret_cast<__attribute__((address_space(3))) s16x4*>(p)); }
DI bf16x8 vcat(s16x4 lo, s16x4 hi) { return __builtin_bit_cast(bf16x8, __builtin_shufflevector(lo, hi, 0, 1, 2, 3, 4, 5, 6, 7)); }
DI bf16x16 plo(const f32x16& p) { return __builtin_bit_cast(bf16x16, __builtin_shufflevector(p, p, 0, 1, 2, 3, 4, 5, 6, 7)); }
DI bf16x16 phi(const f32x16& p) { return __builtin_bit_cast(bf16x16, __builtin_shufflevector(p, p, 8, 9, 10, 11, 12, 13, 14, 15)); }
#define QK(a, b, c) __builtin_amdgcn_mfma_f32_32x32x16_f16(a, b, c, 0, 0, 0)
#define PV(a, b, c) __builtin_amdgcn_smfmac_f32_32x32x32_bf16(a, b, c, SP_IDX, 0, 0)
#define EX4(X, i) do { X[i] = __builtin_amdgcn_exp2f(X[i]); X[i + 1] = __builtin_amdgcn_exp2f(X[i + 1]); \
                       X[i + 2] = __builtin_amdgcn_exp2f(X[i + 2]); X[i + 3] = __builtin_amdgcn_exp2f(X[i + 3]); } while (0)

__global__ __launch_bounds__(512, 2) void qknorm_attn_kernel(const char* __restrict__ Q, const char* __restrict__ K,
                                                             const char* __restrict__ V, const __hip_bfloat16* __restrict__ T,
                                                             char* __restrict__ O) {
    extern __shared__ __attribute__((aligned(16))) char smem[];
    const lds_ptr lds = (lds_ptr)smem;
    const unsigned lds0 = (unsigned)(uintptr_t)lds;
    const int tid = threadIdx.x, lane = tid & 63, hi = lane >> 5, l31 = lane & 31;
    const int wid = __builtin_amdgcn_readfirstlane(tid >> 6);
    const int bx = blockIdx.x, bh = (bx & 7) + 8 * (bx >> 5), quarter = (bx >> 3) & 3;
    const size_t headoff = (size_t)bh * SEQ * ROWB;
    const char* Kh = K + headoff;
    const char* Vh = V + headoff;
    const int qrow0 = quarter * 512 + wid * 64;

    const char* kp = Kh + (size_t)wid * 32 * ROWB + lane * 28;
    Raw7 kraw = load_raw7(kp);
    const char* qp = Q + headoff + (size_t)(qrow0 + l31) * ROWB + hi * 28;
    Raw7 q0 = load_raw7(qp), q1 = load_raw7(qp + 32 * ROWB);
    const char* vsrc = Vh + wid * 2048 + lane * 16;
    const unsigned vdst = lds0 + LDS_V + wid * 2048;
    if (wid < 7) { glds16(vsrc, vdst); glds16(vsrc + 1024, vdst + 1024); }
    const float tval = __bfloat162float(T[bh & (NHEADS - 1)]);
    const bool fast = __builtin_fabsf(tval) <= T_FAST;
    const float tl = tval * LOG2E;

    for (int i = tid; i < ONES_B / 16; i += 512)
        *reinterpret_cast<__attribute__((address_space(3))) u32x4*>(lds + LDS_ONES + i * 16) = (u32x4){0x3f80u, 0u, 0x3f80u, 0u};

    const int kkey = lane >> 1, kh = lane & 1;
    const lds_ptr kwr = lds + LDS_K + wid * 2048 + ((kkey ^ (kh << 2)) * 16 + kh * 512);
    const lds_ptr krd = lds + LDS_K + ((l31 ^ (hi << 2)) * 16 + hi * 512);
    const int vg = (lane >> 4) & 1, vq = (lane & 15) >> 2, vp = lane & 3;
    const bool ones_lane = (vg == 1 && vp == 3);
    const int vbase = (8 * hi + vq) * ROWB + 32 * vg + 8 * vp;
    lds_ptr vrdA = lds + (ones_lane ? LDS_ONES + (hi ? 160 : 224) : LDS_V + vbase);
    lds_ptr vrdB = lds + (ones_lane ? LDS_ONES + (hi ? 160 : 224) : LDS_V + VSLOT + vbase);

    const unsigned kpad = kh ? 0u : 0x3c00u;
#define STAGE_K(slot) do { float ss_ = sumsq7(kraw); \
        ss_ += __builtin_bit_cast(float, __builtin_amdgcn_update_dpp(0, __builtin_bit_cast(int, ss_), 0xB1, 0xf, 0xf, true)); \
        u32x4 f0_, f1_; scale7(kraw, inv_norm(ss_), kpad, f0_, f1_); \
        *reinterpret_cast<__attribute__((address_space(3))) u32x4*>(kwr + (slot) * KSLOT) = f0_; \
        *reinterpret_cast<__attribute__((address_space(3))) u32x4*>(kwr + (slot) * KSLOT + 1024) = f1_; } while (0)

    STAGE_K(0);
    kraw = load_raw7(kp + CHUNK * ROWB);
    f16x8 qa0, qa1, qb0, qb1;
    {
        const float qs = fast ? tl : 1.0f;
        const unsigned qpad = (fast && hi == 0) ? pk_f16(-__builtin_fabsf(tl), 0.f) : 0u;
        u32x4 f0, f1;
        scale7(q0, inv_norm(xhalf_sum(sumsq7(q0))) * qs, qpad, f0, f1);
        qa0 = __builtin_bit_cast(f16x8, f0); qa1 = __builtin_bit_cast(f16x8, f1);
        scale7(q1, inv_norm(xhalf_sum(sumsq7(q1))) * qs, qpad, f0, f1);
        qb0 = __builtin_bit_cast(f16x8, f0); qb1 = __builtin_bit_cast(f16x8, f1);
    }
    f32x16 oa = {}, ob = {};
    const f32x16 zero = {};
#define STAGE_NEXT(cn) do { if ((cn) < NCH) { STAGE_K((cn) & 1); \
        if (wid < 7) { glds16(vsrc + (size_t)(cn) * VSLOT, vdst + ((cn) & 1) * VSLOT); glds16(vsrc + (size_t)(cn) * VSLOT + 1024, vdst + ((cn) & 1) * VSLOT + 1024); } \
        if ((cn) + 1 < NCH) kraw = load_raw7(kp + (size_t)((cn) + 1) * CHUNK * ROWB); } } while (0)

    if (fast) {
        WAIT_BAR();
        f16x8 k0 = lds_rd128(krd), k1 = lds_rd128(krd + 1024);
        s16x4 v0l = lds_rdtr(vrdA), v0h = lds_rdtr(vrdA + 224);
        s16x4 v1l[2], v1h[2];
        v1l[0] = lds_rdtr(vrdA + 896); v1h[0] = lds_rdtr(vrdA + 896 + 224);
        v1l[1] = v1l[0]; v1h[1] = v1h[0];
        STAGE_NEXT(1);
        f32x16 sA, sB, sC, sD;
        sA = QK(k0, qa0, zero); sA = QK(k1, qa1, sA);
        sD = zero;
        lds_ptr krc = krd, krn = krd;
        lds_ptr vrc = vrdA;
#define TILE(PAR, Xa, Xb, Yb, Na, KN, VN) do { \
        SBAR(); Xb = QK(k0, qb0, zero); SBAR(); k0 = lds_rd128(KN); SBAR(); EX4(Xa, 0); \
        SBAR(); Xb = QK(k1, qb1, Xb); SBAR(); k1 = lds_rd128((KN) + 1024); SBAR(); EX4(Xa, 4); \
        SBAR(); ob = PV(vcat(v1l[(PAR) ^ 1], v1h[(PAR) ^ 1]), phi(Yb), ob); SBAR(); EX4(Xa, 8); \
        SBAR(); oa = PV(vcat(v0l, v0h), plo(Xa), oa); SBAR(); EX4(Xa, 12); \
        SBAR(); Na = QK(k0, qa0, zero); SBAR(); v1l[(PAR) ^ 1] = lds_rdtr((VN) + 896); SBAR(); EX4(Xb, 0); \
        SBAR(); Na = QK(k1, qa1, Na); SBAR(); v1h[(PAR) ^ 1] = lds_rdtr((VN) + 896 + 224); SBAR(); EX4(Xb, 4); \
        SBAR(); oa = PV(vcat(v1l[PAR], v1h[PAR]), phi(Xa), oa); SBAR(); EX4(Xb, 8); \
        SBAR(); ob = PV(vcat(v0l, v0h), plo(Xb), ob); SBAR(); v0l = lds_rdtr(VN); v0h = lds_rdtr((VN) + 224); SBAR(); EX4(Xb, 12); \
        SBAR(); } while (0)
        for (int c = 0; c < NCH; ++c) {
            TILE(0, sA, sB, sD, sC, krc + 1 * 2048, vrc + 1 * 1792);
            TILE(1, sC, sD, sB, sA, krc + 2 * 2048, vrc + 2 * 1792);
            TILE(0, sA, sB, sD, sC, krc + 3 * 2048, vrc + 3 * 1792);
            TILE(1, sC, sD, sB, sA, krc + 4 * 2048, vrc + 4 * 1792);
            TILE(0, sA, sB, sD, sC, krc + 5 * 2048, vrc + 5 * 1792);
            TILE(1, sC, sD, sB, sA, krc + 6 * 2048, vrc + 6 * 1792);
            TILE(0, sA, sB, sD, sC, krc + 7 * 2048, vrc + 7 * 1792);
            WAIT_BAR();
            krn = krd + ((c + 1) & 1) * KSLOT;
            lds_ptr vrn = ((c + 1) & 1) ? vrdB : vrdA;
            STAGE_NEXT(c + 2);
            TILE(1, sC, sD, sB, sA, krn, vrn);
            krc = krn; vrc = vrn;
        }
        ob = PV(vcat(v1l[1], v1h[1]), phi(sD), ob);
    } else {
        float ma = -INFINITY, mb = -INFINITY;
        for (int c = 0; c < NCH; ++c) {
            WAIT_BAR();
            STAGE_NEXT(c + 1);
            const lds_ptr krc = krd + (c & 1) * KSLOT;
            const lds_ptr vrc = (c & 1) ? vrdB : vrdA;
            for (int t = 0; t < TPC; ++t) {
                const f16x8 k0 = lds_rd128(krc + t * 2048), k1 = lds_rd128(krc + t * 2048 + 1024);
                const bf16x8 v0 = vcat(lds_rdtr(vrc + t * 1792), lds_rdtr(vrc + t * 1792 + 224));
                const bf16x8 v1 = vcat(lds_rdtr(vrc + t * 1792 + 896), lds_rdtr(vrc + t * 1792 + 896 + 224));
#define SAFE_TILE(q0_, q1_, m_, o_) do { \
                f32x16 s = QK(k0, q0_, zero); s = QK(k1, q1_, s); \
                float mx = -INFINITY; \
                _Pragma("unroll") for (int r = 0; r < 16; ++r) { s[r] *= tl; mx = __builtin_fmaxf(mx, s[r]); } \
                const float mn = __builtin_fmaxf(m_, xhalf_max(mx)); \
                const float al = __builtin_amdgcn_exp2f(m_ - mn); m_ = mn; \
                _Pragma("unroll") for (int r = 0; r < 16; ++r) { s[r] = __builtin_amdgcn_exp2f(s[r] - mn); o_[r] *= al; } \
                o_ = PV(v0, plo(s), o_); o_ = PV(v1, phi(s), o_); } while (0)
                SAFE_TILE(qa0, qa1, ma, oa);
                SAFE_TILE(qb0, qb1, mb, ob);
            }
        }
    }

    {
        const lds_ptr ost = lds + LDS_OST + wid * 3584;
        auto ra = __builtin_amdgcn_permlane32_swap(__float_as_uint(oa[12]), __float_as_uint(oa[12]), false, false);
        auto rb = __builtin_amdgcn_permlane32_swap(__float_as_uint(ob[12]), __float_as_uint(ob[12]), false, false);
        const float ia = 1.0f / __uint_as_float(ra[1]), ib = 1.0f / __uint_as_float(rb[1]);
#pragma unroll
        for (int g = 0; g < 4; ++g) {
            if (g < 3 || hi == 0) {
                *reinterpret_cast<__attribute__((address_space(3))) u32x2*>(ost + l31 * ROWB + 16 * g + 8 * hi) =
                    (u32x2){pk_bf16(oa[4 * g] * ia, oa[4 * g + 1] * ia), pk_bf16(oa[4 * g + 2] * ia, oa[4 * g + 3] * ia)};
                *reinterpret_cast<__attribute__((address_space(3))) u32x2*>(ost + (32 + l31) * ROWB + 16 * g + 8 * hi) =
                    (u32x2){pk_bf16(ob[4 * g] * ib, ob[4 * g + 1] * ib), pk_bf16(ob[4 * g + 2] * ib, ob[4 * g + 3] * ib)};
            }
        }
        asm volatile("s_waitcnt lgkmcnt(0)" ::: "memory");
        char* op = O + headoff + (size_t)qrow0 * ROWB + lane * 16;
#pragma unroll
        for (int i = 0; i < 4; ++i) {
            if (i < 3 || lane < 32)
                *reinterpret_cast<u32x4*>(op + i * 1024) = *reinterpret_cast<const __attribute__((address_space(3))) u32x4*>(ost + lane * 16 + i * 1024);
        }
    }
}

extern "C" void kernel_launch(void* const* d_in, const int* in_sizes, int n_in,
                              void* d_out, int out_size, void* d_ws, size_t ws_size,
                              hipStream_t stream) {
    (void)in_sizes; (void)n_in; (void)out_size; (void)d_ws; (void)ws_size;
    (void)hipFuncSetAttribute((const void*)qknorm_attn_kernel, hipFuncAttributeMaxDynamicSharedMemorySize, LDS_BYTES);
    hipLaunchKernelGGL(qknorm_attn_kernel, dim3(NBH * 4), dim3(512), LDS_BYTES, stream,
                       (const char*)d_in[0], (const char*)d_in[1], (const char*)d_in[2], (const __hip_bfloat16*)d_in[3], (char*)d_out);
}
```

```cpp
#include <hip/hip_runtime.h>
#include <stdint.h>

typedef __bf16 bf16x2 __attribute__((ext_vector_type(2)));
typedef __bf16 bf16x8 __attribute__((ext_vector_type(8)));
typedef __bf16 bf16x16 __attribute__((ext_vector_type(16)));
typedef float f32x2 __attribute__((ext_vector_type(2)));
typedef float f32x16 __attribute__((ext_vector_type(16)));
typedef unsigned u32x2 __attribute__((ext_vector_type(2)));
typedef unsigned u32x3 __attribute__((ext_vector_type(3)));
typedef unsigned u32x4 __attribute__((ext_vector_type(4)));
typedef short s16x4 __attribute__((ext_vector_type(4)));
typedef __attribute__((address_space(3))) char* lds_ptr;
#define LDS_AS __attribute__((address_space(3)))

#define DI __device__ __forceinline__

constexpr int SEQ = 2048, ROWB = 56, NHEADS = 16, NBH = 64;
constexpr int CHUNK = 256, NCH = SEQ / CHUNK, TPC = CHUNK / 32;
constexpr int KSLOT = TPC * 2048;
constexpr int VSLOT = CHUNK * ROWB;
constexpr int LDS_Q = 2 * KSLOT, LDS_V = LDS_Q + 16 * 2048, LDS_ONES = LDS_V + 4 * VSLOT, ONES_B = 8192;
constexpr int LDS_BYTES = LDS_ONES + ONES_B;
constexpr float LOG2E = 1.4426950408889634f;
constexpr int SP_IDX = (int)0xDDDDDDDD;

struct Raw7 { u32x4 a; u32x3 b; };

DI Raw7 load_raw7(const char* p) {
    Raw7 r;
    r.a = *reinterpret_cast<const __attribute__((aligned(4))) u32x4*>(p);
    r.b = *reinterpret_cast<const __attribute__((aligned(4))) u32x3*>(p + 16);
    return r;
}
DI f32x2 unpk(unsigned w) { return (f32x2){__uint_as_float(w << 16), __uint_as_float(w & 0xffff0000u)}; }
DI unsigned pk_bf16(f32x2 v) { return __builtin_bit_cast(unsigned, __builtin_convertvector(v, bf16x2)); }
DI float dot2bf(unsigned a, float c) { return __builtin_amdgcn_fdot2_f32_bf16(__builtin_bit_cast(bf16x2, a), __builtin_bit_cast(bf16x2, a), c, false); }
DI void stage_tile(const Raw7& r, lds_ptr dst) {
    float s0 = dot2bf(r.a[0], 0.f), s1 = dot2bf(r.a[1], 0.f);
    s0 = dot2bf(r.a[2], s0); s1 = dot2bf(r.a[3], s1);
    s0 = dot2bf(r.b[0], s0); s1 = dot2bf(r.b[1], s1);
    s0 = dot2bf(r.b[2], s0) + s1;
    s0 += __builtin_bit_cast(float, __builtin_amdgcn_update_dpp(0, __builtin_bit_cast(int, s0), 0xB1, 0xf, 0xf, true));
    const float sc = __builtin_amdgcn_rcpf(__builtin_amdgcn_sqrtf(s0) + 1e-6f);
    *reinterpret_cast<LDS_AS u32x4*>(dst) =
        (u32x4){pk_bf16(unpk(r.a[0]) * sc), pk_bf16(unpk(r.a[1]) * sc), pk_bf16(unpk(r.a[2]) * sc), pk_bf16(unpk(r.a[3]) * sc)};
    *reinterpret_cast<LDS_AS u32x4*>(dst + 1024) = (u32x4){pk_bf16(unpk(r.b[0]) * sc), pk_bf16(unpk(r.b[1]) * sc), pk_bf16(unpk(r.b[2]) * sc), 0u};
}
DI float xhalf_max(float x) {
    auto rr = __builtin_amdgcn_permlane32_swap(__float_as_uint(x), __float_as_uint(x), false, false);
    return __builtin_fmaxf(__uint_as_float(rr[0]), __uint_as_float(rr[1]));
}
DI void glds16x2(const void* g, unsigned lds_base) {
    unsigned sv;
    asm volatile("s_mov_b32 %0, m0\n\ts_mov_b32 m0, %2\n\ts_nop 0\n\tglobal_load_lds_dwordx4 %1, off\n\t"
                 "global_load_lds_dwordx4 %1, off offset:1024\n\ts_mov_b32 m0, %0"
                 : "=&s"(sv) : "v"(g), "s"(lds_base) : "memory");
}
#define WAIT_BAR() asm volatile("s_waitcnt vmcnt(0) lgkmcnt(0)\n\ts_barrier" ::: "memory")

DI bf16x8 lds_rd128(lds_ptr p) { return *reinterpret_cast<const LDS_AS bf16x8*>(p); }
DI s16x4 lds_rdtr(lds_ptr p) { return __builtin_amdgcn_ds_read_tr16_b64_v4i16(reinterpret_cast<LDS_AS s16x4*>(p)); }
DI bf16x8 vcat(s16x4 lo, s16x4 hi) { return __builtin_bit_cast(bf16x8, __builtin_shufflevector(lo, hi, 0, 1, 2, 3, 4, 5, 6, 7)); }
DI bf16x16 plo(const f32x16& p) { return __builtin_bit_cast(bf16x16, __builtin_shufflevector(p, p, 0, 1, 2, 3, 4, 5, 6, 7)); }
DI bf16x16 phi(const f32x16& p) { return __builtin_bit_cast(bf16x16, __builtin_shufflevector(p, p, 8, 9, 10, 11, 12, 13, 14, 15)); }
#define QK(a, b, c) __builtin_amdgcn_mfma_f32_32x32x16_bf16(a, b, c, 0, 0, 0)
#define PV(a, b, c) __builtin_amdgcn_smfmac_f32_32x32x32_bf16(a, b, c, SP_IDX, 0, 0)

__global__ __attribute__((aligned(4096))) __launch_bounds__(512, 2) void qknorm_attn_kernel(const char* __restrict__ Q, const char* __restrict__ K,
                                                             const char* __restrict__ V, const unsigned* __restrict__ T,
                                                             char* __restrict__ O) {
    extern __shared__ __attribute__((aligned(16))) char smem[];
    int tid = threadIdx.x, done;
    asm volatile(
        "s_load_dwordx8 s[48:55], %[ka], 0x0\n\t"
        "s_load_dwordx2 s[56:57], %[ka], 0x20\n\t"
        "s_branch 10f\n\t"
        "51:\n\t"
        "s_sleep 1\n\t"
        "ds_read_b32 v238, v236 offset:7172\n\t"
        "s_waitcnt lgkmcnt(0)\n\t"
        "v_cmp_ne_u32 vcc, 0, v238\n\t"
        "s_andn2_b32 s41, s42, vcc_lo\n\t"
        "s_cbranch_scc1 51b\n\t"
        "s_branch 52f\n\t"
        "61:\n\t"
        "s_sleep 1\n\t"
        "ds_read_b32 v238, v236 offset:7172\n\t"
        "s_waitcnt lgkmcnt(0)\n\t"
        "v_cmp_ne_u32 vcc, 0, v238\n\t"
        "s_andn2_b32 s41, s43, vcc_lo\n\t"
        "s_cbranch_scc1 61b\n\t"
        "s_branch 62f\n\t"
        "21:\n\t"
        "s_sleep 1\n\t"
        "ds_read_b32 v238, v236 offset:7172\n\t"
        "s_waitcnt lgkmcnt(0)\n\t"
        "v_cmp_ne_u32 vcc, 0, v238\n\t"
        "s_andn2_b32 s41, s66, vcc_lo\n\t"
        "s_cbranch_scc1 21b\n\t"
        "s_branch 22f\n\t"
        "10:\n\t"
        "v_readfirstlane_b32 s58, %[tid]\n\t"
        "v_and_b32 v116, 31, %[tid]\n\t"
        "v_bfe_u32 v117, %[tid], 5, 1\n\t"
        "s_and_b32 s59, %[bx], 7\n\t"
        "s_lshr_b32 s61, %[bx], 5\n\t"
        "s_lshl3_add_u32 s59, s61, s59\n\t"
        "v_lshl_add_u32 v121, v116, 1, v117\n\t"
        "s_mul_i32 s60, s59, 0x1c000\n\t"
        "s_mul_i32 s61, s58, 28\n\t"
        "v_mul_u32_u24 v118, 28, v121\n\t"
        "s_add_u32 s62, s61, s60\n\t"
        "s_and_b32 s69, %[bx], 24\n\t"
        "v_add_u32 v119, s62, v118\n\t"
        "s_mulk_i32 s69, 0xe00\n\t"
        "s_add_u32 s69, s69, s60\n\t"
        "s_lshl1_add_u32 s62, s61, s69\n\t"
        "v_and_b32 v121, 63, %[tid]\n\t"
        "v_add_u32 v234, s62, v118\n\t"
        "s_min_u32 s61, s61, 0x3000\n\t"
        "v_lshlrev_b32 v236, 4, v121\n\t"
        "s_add_u32 s63, s61, 73728\n\t"
        "s_add_u32 s61, s61, s60\n\t"
        "v_add_u32 v225, s61, v236\n\t"
        "s_mov_b32 m0, s63\n\t"
        "s_and_b32 s64, s59, 14\n\t"
        "s_lshl_b32 s64, s64, 1\n\t"
        "v_mov_b32 v128, 0x3f80\n\t"
        "v_mov_b32 v129, 0\n\t"
        "v_lshlrev_b32 v122, 4, %[tid]\n\t"
        "ds_write2_b64 v122, v[128:129], v[128:129] offset1:1\n\t"
        "v_lshlrev_b32 v126, 6, v117\n\t"
        "s_lshl_b32 s62, s58, 5\n\t"
        "v_xor_b32 v120, v126, v236\n\t"
        "v_add_u32 v124, -1, v117\n\t"
        "v_add_u32 v240, s62, v120\n\t"
        "s_waitcnt lgkmcnt(0)\n\t"
        "global_load_dwordx4 v[132:135], v119, s[50:51] offset:0\n\t"
        "global_load_dwordx3 v[136:138], v119, s[50:51] offset:16\n\t"
        "s_load_dword s40, s[54:55], s64\n\t"
        "s_lshr_b32 s61, s58, 2\n\t"
        "v_mov_b32 v223, 0xdddddddd\n\t"
        "v_mov_b32 v237, s61\n\t"
        "s_mov_b32 s67, 0xffff0000\n\t"
        "s_mov_b32 s68, 0x358637bd\n\t"
        "v_mov_b32 v2, 0\n\t"
        "v_mov_b32 v3, 0\n\t"
        "v_and_b32 v32, 0x45800000, v124\n\t"
        "v_xor_b32 v32, 0x45800000, v32\n\t"
        "v_and_b32 v33, 0x7a60000, v124\n\t"
        "v_xor_b32 v33, 0xc2260000, v33\n\t"
        "v_mfma_i32_32x32x4_2b_i8 v[36:67], v129, v129, 0\n\t"
        "v_and_b32 v121, 1, %[tid]\n\t"
        "v_and_b32 v122, 62, %[tid]\n\t"
        "v_mul_u32_u24 v121, 0x240, v121\n\t"
        "v_lshlrev_b32 v122, 3, v122\n\t"
        "v_xad_u32 v222, v121, v122, s62\n\t"
        "v_and_b32 v220, 0x27ff, v240\n\t"
        "v_mul_u32_u24 v121, 28, %[tid]\n\t"
        "s_lshr_b32 s65, s58, 6\n\t"
        "v_add_u32 v224, s60, v121\n\t"
        "v_bfe_u32 v121, %[tid], 2, 2\n\t"
        "v_mfma_i32_32x32x4_2b_i8 v[68:99], v129, v129, 0\n\t"
        "v_and_b32 v123, 19, %[tid]\n\t"
        "v_lshl_add_u32 v121, v117, 3, v121\n\t"
        "v_cmp_eq_u32 vcc, 19, v123\n\t"
        "v_lshrrev_b32 v122, 2, v123\n\t"
        "v_mul_u32_u24 v121, 7, v121\n\t"
        "v_or_b32 v122, v122, v123\n\t"
        "v_and_b32 v122, 7, v122\n\t"
        "v_add_u32 v121, v121, v122\n\t"
        "v_lshlrev_b32 v121, 3, v121\n\t"
        "v_add_u32 v121, 73728, v121\n\t"
        "v_sub_u32 v122, 0xe0, v126\n\t"
        "v_cndmask_b32 v127, v121, v122, vcc\n\t"
        "v_mov_b32 v121, 0x700\n\t"
        "s_and_b32 s66, s65, 4\n\t"
        "v_cndmask_b32 v125, v121, v129, vcc\n\t"
        "v_mad_u32_u24 v244, v125, s65, v127\n\t"
        "s_xor_b32 s61, s65, 1\n\t"
        "v_xor_b32 v241, 0x800, v240\n\t"
        "v_mad_u32_u24 v245, v125, s61, v127\n\t"
        "s_xor_b32 s61, s65, 2\n\t"
        "v_xor_b32 v242, 0x1000, v240\n\t"
        "v_mad_u32_u24 v246, v125, s61, v127\n\t"
        "s_xor_b32 s61, s65, 3\n\t"
        "v_xor_b32 v243, 0x1800, v240\n\t"
        "v_mad_u32_u24 v247, v125, s61, v127\n\t"
        "v_mad_u32_u24 v226, v125, s66, v127\n\t"
        "v_lshlrev_b32 v227, 3, v125\n\t"
        "global_load_dwordx4 v[140:143], v234, s[48:49] offset:0\n\t"
        "global_load_dwordx3 v[144:146], v234, s[48:49] offset:16\n\t"
        "global_load_lds_dwordx4 v225, s[52:53]\n\t"
        "global_load_lds_dwordx4 v225, s[52:53] offset:1024\n\t"
        "v_add_u32 v232, s62, v240\n\t"
        "v_add_u32 v221, v226, v227\n\t"
        "v_xor_b32 v233, 0x4000, v232\n\t"
        "s_lshl_b32 s42, 15, s66\n\t"
        "s_xor_b32 s61, s65, 12\n\t"
        "s_lshl_b32 s43, 1, s61\n\t"
        "s_lshl_b32 s66, s42, 8\n\t"
        "s_lshl_b32 s62, s58, 7\n\t"
        "v_mul_u32_u24 v121, 56, v116\n\t"
        "v_add_u32 v229, s62, v236\n\t"
        "v_lshl_add_u32 v121, v117, 3, v121\n\t"
        "v_xor_b32 v228, 0x8000, v229\n\t"
        "v_add_u32 v230, s62, v121\n\t"
        "s_xor_b32 s61, s65, 4\n\t"
        "s_mulk_i32 s61, 0xe00\n\t"
        "s_add_u32 s61, s61, s69\n\t"
        "v_add_u32 v231, s61, v236\n\t"
        "v_mov_b32 v235, 0x3fb8aa3b\n\t"
        "s_movk_i32 s47, 0x3800\n\t"
        "v_lshlrev_b32 v121, 4, %[tid]\n\t"
        "s_mov_b32 s36, 3\n\t"
        "s_mov_b32 s37, 48\n\t"
        "v_lshlrev_b32 v121, v121, v128\n\t"
        "v_cndmask_b32 v248, 0, v121, s[36:37]\n\t"
        "s_lshl_b64 s[36:37], s[36:37], 2\n\t"
        "v_cndmask_b32 v249, 0, v121, s[36:37]\n\t"
        "s_lshl_b64 s[36:37], s[36:37], 6\n\t"
        "v_cndmask_b32 v250, 0, v121, s[36:37]\n\t"
        "s_lshl_b64 s[36:37], s[36:37], 2\n\t"
        "v_cndmask_b32 v251, 0, v121, s[36:37]\n\t"
        "s_lshl_b64 s[36:37], s[36:37], 6\n\t"
        "v_cndmask_b32 v252, 0, v121, s[36:37]\n\t"
        "s_lshl_b64 s[36:37], s[36:37], 2\n\t"
        "v_cndmask_b32 v253, 0, v121, s[36:37]\n\t"
        "s_lshl_b64 s[36:37], s[36:37], 6\n\t"
        "v_cndmask_b32 v254, 0, v121, s[36:37]\n\t"
        "s_lshl_b64 s[36:37], s[36:37], 2\n\t"
        "v_cndmask_b32 v255, 0, v121, s[36:37]\n\t"
        "v_mov_b32 v203, 0\n\t"
        "s_and_b32 s59, s59, 1\n\t"
        "s_lshl_b32 s59, s59, 4\n\t"
        "s_or_b32 s60, s59, 0xf0000\n\t"
        "s_add_u32 s45, s63, s47\n\t"
        "s_mov_b32 %[ok], 0\n\t"
        "s_mov_b32 s44, 1\n\t"
        "s_mov_b32 s46, 2\n\t"
        "s_add_u32 s50, s50, s47\n\t"
        "s_addc_u32 s51, s51, 0\n\t"
        "s_add_u32 s52, s52, s47\n\t"
        "s_addc_u32 s53, s53, 0\n\t"
        "s_setprio 3\n\t"
        "s_barrier\n\t"
        "s_waitcnt vmcnt(4)\n\t"
        "v_dot2c_f32_bf16 v2, v132, v132\n\t"
        "v_dot2c_f32_bf16 v3, v133, v133\n\t"
        "v_dot2c_f32_bf16 v2, v134, v134\n\t"
        "v_dot2c_f32_bf16 v3, v135, v135\n\t"
        "v_dot2c_f32_bf16 v2, v136, v136\n\t"
        "v_dot2c_f32_bf16 v3, v137, v137\n\t"
        "v_dot2c_f32_bf16 v2, v138, v138\n\t"
        "v_lshlrev_b32 v148, 16, v132\n\t"
        "v_and_b32 v149, s67, v132\n\t"
        "v_lshlrev_b32 v150, 16, v133\n\t"
        "v_and_b32 v151, s67, v133\n\t"
        "v_add_f32 v139, v2, v3\n\t"
        "v_add_f32 v2, v2, v3\n\t"
        "v_lshlrev_b32 v152, 16, v134\n\t"
        "v_and_b32 v153, s67, v134\n\t"
        "v_permlane32_swap_b32 v2, v139\n\t"
        "v_add_f32 v2, v2, v139\n\t"
        "v_sqrt_f32 v2, v2\n\t"
        "v_lshlrev_b32 v154, 16, v135\n\t"
        "v_and_b32 v155, s67, v135\n\t"
        "v_add_f32 v2, s68, v2\n\t"
        "v_rcp_f32 v2, v2\n\t"
        "v_lshlrev_b32 v156, 16, v136\n\t"
        "v_and_b32 v157, s67, v136\n\t"
        "v_lshlrev_b32 v158, 16, v137\n\t"
        "v_and_b32 v159, s67, v137\n\t"
        "v_lshlrev_b32 v160, 16, v138\n\t"
        "v_and_b32 v161, s67, v138\n\t"
        "v_pk_mul_f32 v[148:149], v[2:3], v[148:149] op_sel_hi:[0,1]\n\t"
        "v_pk_mul_f32 v[150:151], v[2:3], v[150:151] op_sel_hi:[0,1]\n\t"
        "v_pk_mul_f32 v[152:153], v[2:3], v[152:153] op_sel_hi:[0,1]\n\t"
        "v_pk_mul_f32 v[154:155], v[2:3], v[154:155] op_sel_hi:[0,1]\n\t"
        "v_cvt_pk_bf16_f32 v164, v148, v149\n\t"
        "v_cvt_pk_bf16_f32 v165, v150, v151\n\t"
        "v_pk_mul_f32 v[156:157], v[2:3], v[156:157] op_sel_hi:[0,1]\n\t"
        "v_cvt_pk_bf16_f32 v166, v152, v153\n\t"
        "v_pk_mul_f32 v[158:159], v[2:3], v[158:159] op_sel_hi:[0,1]\n\t"
        "v_cvt_pk_bf16_f32 v167, v154, 1.0\n\t"
        "v_pk_mul_f32 v[160:161], v[2:3], v[160:161] op_sel_hi:[0,1]\n\t"
        "ds_write_b128 v240, v[164:167] offset:24576\n\t"
        "v_cvt_pk_bf16_f32 v168, v155, v156\n\t"
        "v_cvt_pk_bf16_f32 v169, v157, v158\n\t"
        "v_cvt_pk_bf16_f32 v170, v159, v160\n\t"
        "v_cvt_pk_bf16_f32 v171, v161, 1.0\n\t"
        "ds_write_b128 v240, v[168:171] offset:25600\n\t"
        "ds_write_b32 v237, v223 offset:7172\n\t"
        "s_setprio 2\n\t"
        "v_mov_b32 v2, 0\n\t"
        "v_mov_b32 v3, 0\n\t"
        "s_waitcnt lgkmcnt(0)\n\t"
        "s_bfe_u32 s41, s40, s60\n\t"
        "s_lshr_b32 s40, s40, s59\n\t"
        "s_lshl_b32 s40, s40, 16\n\t"
        "s_cmpk_le_u32 s41, 0x4120\n\t"
        "s_cbranch_scc0 9f\n\t"
        "v_mul_f32 v235, s40, v235\n\t"
        "s_setprio 3\n\t"
        "s_waitcnt vmcnt(2)\n\t"
        "v_dot2c_f32_bf16 v2, v140, v140\n\t"
        "v_dot2c_f32_bf16 v3, v141, v141\n\t"
        "v_dot2c_f32_bf16 v2, v142, v142\n\t"
        "v_dot2c_f32_bf16 v3, v143, v143\n\t"
        "v_dot2c_f32_bf16 v2, v144, v144\n\t"
        "v_dot2c_f32_bf16 v3, v145, v145\n\t"
        "v_dot2c_f32_bf16 v2, v146, v146\n\t"
        "v_lshlrev_b32 v204, 16, v140\n\t"
        "v_and_b32 v205, s67, v140\n\t"
        "v_lshlrev_b32 v206, 16, v141\n\t"
        "ds_read_b32 v238, v236 offset:7172\n\t"
        "v_and_b32 v207, s67, v141\n\t"
        "v_add_f32 v147, v2, v3\n\t"
        "v_add_f32 v2, v2, v3\n\t"
        "v_lshlrev_b32 v208, 16, v142\n\t"
        "v_and_b32 v209, s67, v142\n\t"
        "v_permlane32_swap_b32 v2, v147\n\t"
        "v_add_f32 v2, v2, v147\n\t"
        "v_sqrt_f32 v2, v2\n\t"
        "v_lshlrev_b32 v210, 16, v143\n\t"
        "v_and_b32 v211, s67, v143\n\t"
        "v_add_f32 v2, s68, v2\n\t"
        "v_rcp_f32 v2, v2\n\t"
        "v_lshlrev_b32 v212, 16, v144\n\t"
        "v_and_b32 v213, s67, v144\n\t"
        "v_mul_f32 v2, v235, v2\n\t"
        "v_lshlrev_b32 v214, 16, v145\n\t"
        "v_and_b32 v215, s67, v145\n\t"
        "v_lshlrev_b32 v216, 16, v146\n\t"
        "v_and_b32 v217, s67, v146\n\t"
        "v_pk_mul_f32 v[204:205], v[2:3], v[204:205] op_sel_hi:[0,1]\n\t"
        "v_pk_mul_f32 v[206:207], v[2:3], v[206:207] op_sel_hi:[0,1]\n\t"
        "v_pk_mul_f32 v[208:209], v[2:3], v[208:209] op_sel_hi:[0,1]\n\t"
        "v_pk_mul_f32 v[210:211], v[2:3], v[210:211] op_sel_hi:[0,1]\n\t"
        "v_cvt_pk_bf16_f32 v4, v204, v205\n\t"
        "v_cvt_pk_bf16_f32 v5, v206, v207\n\t"
        "v_pk_mul_f32 v[212:213], v[2:3], v[212:213] op_sel_hi:[0,1]\n\t"
        "s_waitcnt lgkmcnt(0)\n\t"
        "v_cmp_ne_u32 vcc, 0, v238\n\t"
        "s_andn2_b32 s41, s42, vcc_lo\n\t"
        "s_cbranch_scc1 51b\n\t"
        "52:\n\t"
        "ds_read_b128 v[176:179], v241 offset:25600\n\t"
        "ds_read_b128 v[172:175], v241 offset:24576\n\t"
        "v_cvt_pk_bf16_f32 v6, v208, v209\n\t"
        "v_pk_mul_f32 v[214:215], v[2:3], v[214:215] op_sel_hi:[0,1]\n\t"
        "v_cvt_pk_bf16_f32 v7, v210, v32\n\t"
        "v_pk_mul_f32 v[216:217], v[2:3], v[216:217] op_sel_hi:[0,1]\n\t"
        "v_cvt_pk_bf16_f32 v8, v211, v212\n\t"
        "v_cvt_pk_bf16_f32 v9, v213, v214\n\t"
        "v_cvt_pk_bf16_f32 v10, v215, v216\n\t"
        "v_mfma_f32_32x32x16_bf16 v[100:115], v[164:167], v[4:7], 0\n\t"
        "v_cvt_pk_bf16_f32 v11, v217, v33\n\t"
        "ds_write_b128 v232, v[4:7] offset:40960\n\t"
        "ds_write_b128 v232, v[8:11] offset:41984\n\t"
        "v_mfma_f32_32x32x16_bf16 v[100:115], v[168:171], v[8:11], v[100:115]\n\t"
        "s_waitcnt lgkmcnt(3)\n\t"
        "ds_read_b128 v[164:167], v242 offset:24576\n\t"
        "ds_read_b128 v[168:171], v242 offset:25600\n\t"
        "s_waitcnt vmcnt(0)\n\t"
        "global_load_dwordx4 v[196:199], v234, s[48:49] offset:1792\n\t"
        "global_load_dwordx3 v[200:202], v234, s[48:49] offset:1808\n\t"
        "ds_write_b32 v237, v223 offset:7300\n\t"
        "ds_read_b64_tr_b16 v[180:181], v244 offset:0\n\t"
        "ds_read_b64_tr_b16 v[182:183], v244 offset:224\n\t"
        "ds_read_b64_tr_b16 v[184:185], v244 offset:896\n\t"
        "ds_read_b64_tr_b16 v[186:187], v244 offset:1120\n\t"
        "s_setprio 2\n\t"
        "s_nop 1\n\t"
        "v_mfma_f32_32x32x16_bf16 v[116:131], v[176:179], v[8:11], 0\n\t"
        "s_waitcnt lgkmcnt(9)\n\t"
        "ds_read_b32 v238, v236 offset:7172\n\t"
        "v_exp_f32 v100, v100\n\t"
        "v_exp_f32 v101, v101\n\t"
        "v_exp_f32 v102, v102\n\t"
        "v_exp_f32 v103, v103\n\t"
        "v_mfma_f32_32x32x16_bf16 v[116:131], v[172:175], v[4:7], v[116:131]\n\t"
        "v_exp_f32 v104, v104\n\t"
        "v_exp_f32 v105, v105\n\t"
        "v_exp_f32 v106, v106\n\t"
        "v_exp_f32 v107, v107\n\t"
        "s_waitcnt lgkmcnt(0)\n\t"
        "v_cmp_ne_u32 vcc, 0, v238\n\t"
        "s_andn2_b32 s41, s66, vcc_lo\n\t"
        "s_cbranch_scc1 21b\n\t"
        "22:\n\t"
        "ds_read_b128 v[176:179], v243 offset:25600\n\t"
        "ds_read_b64_tr_b16 v[188:189], v245 offset:0\n\t"
        "v_exp_f32 v108, v108\n\t"
        "v_exp_f32 v109, v109\n\t"
        "v_exp_f32 v110, v110\n\t"
        "v_exp_f32 v111, v111\n\t"
        "v_smfmac_f32_32x32x32_bf16 v[36:51], v[180:183], v[100:107], v223\n\t"
        "ds_read_b128 v[172:175], v243 offset:24576\n\t"
        "ds_read_b64_tr_b16 v[190:191], v245 offset:224\n\t"
        "v_exp_f32 v112, v112\n\t"
        "v_exp_f32 v113, v113\n\t"
        "v_exp_f32 v114, v114\n\t"
        "v_exp_f32 v115, v115\n\t"
        "v_mfma_f32_32x32x16_bf16 v[132:147], v[168:171], v[8:11], 0\n\t"
        "ds_read_b64_tr_b16 v[192:193], v245 offset:896\n\t"
        "v_exp_f32 v116, v116\n\t"
        "v_exp_f32 v117, v117\n\t"
        "v_exp_f32 v118, v118\n\t"
        "v_exp_f32 v119, v119\n\t"
        "v_mfma_f32_32x32x16_bf16 v[132:147], v[164:167], v[4:7], v[132:147]\n\t"
        "ds_read_b64_tr_b16 v[194:195], v245 offset:1120\n\t"
        "v_exp_f32 v120, v120\n\t"
        "v_exp_f32 v121, v121\n\t"
        "v_exp_f32 v122, v122\n\t"
        "v_exp_f32 v123, v123\n\t"
        "v_smfmac_f32_32x32x32_bf16 v[36:51], v[184:187], v[108:115], v223\n\t"
        "s_waitcnt lgkmcnt(2)\n\t"
        "ds_read_b64_tr_b16 v[180:181], v246 offset:0\n\t"
        "ds_read_b32 v238, v236 offset:7172\n\t"
        "v_exp_f32 v124, v124\n\t"
        "v_exp_f32 v125, v125\n\t"
        "v_exp_f32 v126, v126\n\t"
        "v_exp_f32 v127, v127\n\t"
        "v_smfmac_f32_32x32x32_bf16 v[36:51], v[188:191], v[116:123], v223\n\t"
        "ds_read_b64_tr_b16 v[182:183], v246 offset:224\n\t"
        "s_setprio 1\n\t"
        "v_exp_f32 v128, v128\n\t"
        "v_exp_f32 v129, v129\n\t"
        "v_exp_f32 v130, v130\n\t"
        "v_exp_f32 v131, v131\n\t"
        "v_mfma_f32_32x32x16_bf16 v[148:163], v[176:179], v[8:11], 0\n\t"
        "ds_read_b64_tr_b16 v[184:185], v246 offset:896\n\t"
        "s_waitcnt lgkmcnt(2)\n\t"
        "v_cmp_ne_u32 vcc, 0, v238\n\t"
        "s_andn2_b32 s41, s43, vcc_lo\n\t"
        "s_cbranch_scc1 61b\n\t"
        "62:\n\t"
        "ds_read_b128 v[24:27], v233 offset:41984\n\t"
        "ds_read_b128 v[20:23], v233 offset:40960\n\t"
        "v_exp_f32 v132, v132\n\t"
        "v_exp_f32 v133, v133\n\t"
        "v_exp_f32 v134, v134\n\t"
        "v_exp_f32 v135, v135\n\t"
        "v_mfma_f32_32x32x16_bf16 v[148:163], v[172:175], v[4:7], v[148:163]\n\t"
        "ds_read_b64_tr_b16 v[186:187], v246 offset:1120\n\t"
        "v_exp_f32 v136, v136\n\t"
        "v_exp_f32 v137, v137\n\t"
        "v_exp_f32 v138, v138\n\t"
        "v_exp_f32 v139, v139\n\t"
        "v_smfmac_f32_32x32x32_bf16 v[36:51], v[192:195], v[124:131], v223\n\t"
        "s_waitcnt lgkmcnt(4)\n\t"
        "ds_read_b64_tr_b16 v[188:189], v247 offset:0\n\t"
        "s_waitcnt vmcnt(0)\n\t"
        "s_setprio 3\n\t"
        "v_exp_f32 v140, v140\n\t"
        "v_exp_f32 v141, v141\n\t"
        "v_exp_f32 v142, v142\n\t"
        "v_exp_f32 v143, v143\n\t"
        "v_dot2_f32_bf16 v2, v196, v196, 0\n\t"
        "v_dot2_f32_bf16 v3, v197, v197, 0\n\t"
        "v_dot2_f32_bf16 v2, v198, v198, v2\n\t"
        "v_dot2_f32_bf16 v3, v199, v199, v3\n\t"
        "v_smfmac_f32_32x32x32_bf16 v[36:51], v[180:183], v[132:139], v223\n\t"
        "s_waitcnt lgkmcnt(3)\n\t"
        "ds_read_b64_tr_b16 v[190:191], v247 offset:224\n\t"
        "v_exp_f32 v144, v144\n\t"
        "v_exp_f32 v145, v145\n\t"
        "v_exp_f32 v146, v146\n\t"
        "v_exp_f32 v147, v147\n\t"
        "v_dot2_f32_bf16 v2, v200, v200, v2\n\t"
        "v_dot2_f32_bf16 v3, v201, v201, v3\n\t"
        "v_dot2_f32_bf16 v2, v202, v202, v2\n\t"
        "v_mfma_f32_32x32x16_bf16 v[100:115], v[168:171], v[24:27], 0\n\t"
        "s_waitcnt lgkmcnt(3)\n\t"
        "ds_read_b64_tr_b16 v[192:193], v247 offset:896\n\t"
        "s_setprio 1\n\t"
        "v_exp_f32 v148, v148\n\t"
        "v_exp_f32 v149, v149\n\t"
        "v_exp_f32 v150, v150\n\t"
        "v_exp_f32 v151, v151\n\t"
        "v_add_f32 v239, v2, v3\n\t"
        "v_add_f32 v2, v2, v3\n\t"
        "v_mfma_f32_32x32x16_bf16 v[100:115], v[164:167], v[20:23], v[100:115]\n\t"
        "ds_read_b64_tr_b16 v[194:195], v247 offset:1120\n\t"
        "v_exp_f32 v152, v152\n\t"
        "v_exp_f32 v153, v153\n\t"
        "v_exp_f32 v154, v154\n\t"
        "v_mfma_f32_32x32x16_bf16 v[204:219], v[248:251], v[196:199], 0\n\t"
        "s_waitcnt lgkmcnt(4)\n\t"
        "v_exp_f32 v155, v155\n\t"
        "v_permlane32_swap_b32 v2, v239\n\t"
        "v_smfmac_f32_32x32x32_bf16 v[36:51], v[184:187], v[140:147], v223\n\t"
        "s_waitcnt lgkmcnt(2)\n\t"
        "ds_read_b128 v[168:171], v240 offset:25600\n\t"
        "v_exp_f32 v156, v156\n\t"
        "v_exp_f32 v157, v157\n\t"
        "v_exp_f32 v158, v158\n\t"
        "v_exp_f32 v159, v159\n\t"
        "v_add_f32 v2, v2, v239\n\t"
        "v_sqrt_f32 v2, v2\n\t"
        "v_smfmac_f32_32x32x32_bf16 v[36:51], v[188:191], v[148:155], v223\n\t"
        "ds_read_b128 v[164:167], v240 offset:24576\n\t"
        "v_exp_f32 v160, v160\n\t"
        "v_exp_f32 v161, v161\n\t"
        "v_exp_f32 v162, v162\n\t"
        "v_mfma_f32_32x32x16_bf16 v[204:219], v[252:255], v[200:203], v[204:219]\n\t"
        "v_exp_f32 v163, v163\n\t"
        "v_add_f32 v2, s68, v2\n\t"
        "v_mfma_f32_32x32x16_bf16 v[116:131], v[176:179], v[24:27], 0\n\t"
        "v_exp_f32 v100, v100\n\t"
        "v_exp_f32 v101, v101\n\t"
        "v_exp_f32 v102, v102\n\t"
        "v_exp_f32 v103, v103\n\t"
        "v_rcp_f32 v2, v2\n\t"
        "v_mfma_f32_32x32x16_bf16 v[116:131], v[172:175], v[20:23], v[116:131]\n\t"
        "s_waitcnt lgkmcnt(2)\n\t"
        "v_exp_f32 v104, v104\n\t"
        "v_exp_f32 v105, v105\n\t"
        "v_exp_f32 v106, v106\n\t"
        "v_exp_f32 v107, v107\n\t"
        "v_mul_f32 v2, v235, v2\n\t"
        "v_smfmac_f32_32x32x32_bf16 v[36:51], v[192:195], v[156:163], v223\n\t"
        "ds_read_b128 v[176:179], v241 offset:25600\n\t"
        "s_mov_b32 m0, s45\n\t"
        "global_load_dwordx4 v[196:199], v224, s[50:51] offset:0\n\t"
        "global_load_lds_dwordx4 v225, s[52:53]\n\t"
        "global_load_lds_dwordx4 v225, s[52:53] offset:1024\n\t"
        "global_load_dwordx3 v[200:202], v224, s[50:51] offset:16\n\t"
        "s_setprio 3\n\t"
        "v_exp_f32 v108, v108\n\t"
        "v_exp_f32 v109, v109\n\t"
        "v_exp_f32 v110, v110\n\t"
        "v_exp_f32 v111, v111\n\t"
        "v_pk_mul_f32 v[204:205], v[2:3], v[204:205] op_sel_hi:[0,1]\n\t"
        "v_pk_mul_f32 v[206:207], v[2:3], v[206:207] op_sel_hi:[0,1]\n\t"
        "v_pk_mul_f32 v[208:209], v[2:3], v[208:209] op_sel_hi:[0,1]\n\t"
        "v_pk_mul_f32 v[210:211], v[2:3], v[210:211] op_sel_hi:[0,1]\n\t"
        "v_smfmac_f32_32x32x32_bf16 v[68:83], v[180:183], v[100:107], v223\n\t"
        "s_waitcnt lgkmcnt(2)\n\t"
        "ds_read_b128 v[172:175], v241 offset:24576\n\t"
        "v_exp_f32 v112, v112\n\t"
        "v_exp_f32 v113, v113\n\t"
        "v_exp_f32 v114, v114\n\t"
        "v_exp_f32 v115, v115\n\t"
        "v_pk_mul_f32 v[212:213], v[2:3], v[212:213] op_sel_hi:[0,1]\n\t"
        "v_pk_mul_f32 v[214:215], v[2:3], v[214:215] op_sel_hi:[0,1]\n\t"
        "v_pk_mul_f32 v[216:217], v[2:3], v[216:217] op_sel_hi:[0,1]\n\t"
        "v_mfma_f32_32x32x16_bf16 v[132:147], v[168:171], v[24:27], 0\n\t"
        "s_waitcnt lgkmcnt(2)\n\t"
        "ds_read_b64_tr_b16 v[180:181], v244 offset:0\n\t"
        "s_setprio 1\n\t"
        "v_exp_f32 v116, v116\n\t"
        "v_exp_f32 v117, v117\n\t"
        "v_exp_f32 v118, v118\n\t"
        "v_exp_f32 v119, v119\n\t"
        "v_cvt_pk_bf16_f32 v12, v204, v205\n\t"
        "v_cvt_pk_bf16_f32 v13, v206, v207\n\t"
        "v_mfma_f32_32x32x16_bf16 v[132:147], v[164:167], v[20:23], v[132:147]\n\t"
        "ds_read_b64_tr_b16 v[182:183], v244 offset:224\n\t"
        "v_exp_f32 v120, v120\n\t"
        "v_exp_f32 v121, v121\n\t"
        "v_exp_f32 v122, v122\n\t"
        "v_exp_f32 v123, v123\n\t"
        "v_cvt_pk_bf16_f32 v14, v208, v209\n\t"
        "v_cvt_pk_bf16_f32 v15, v210, v32\n\t"
        "v_cvt_pk_bf16_f32 v16, v211, v212\n\t"
        "v_smfmac_f32_32x32x32_bf16 v[68:83], v[184:187], v[108:115], v223\n\t"
        "ds_write_b128 v232, v[12:15] offset:43008\n\t"
        "v_exp_f32 v124, v124\n\t"
        "v_exp_f32 v125, v125\n\t"
        "v_exp_f32 v126, v126\n\t"
        "v_exp_f32 v127, v127\n\t"
        "v_cvt_pk_bf16_f32 v17, v213, v214\n\t"
        "v_cvt_pk_bf16_f32 v18, v215, v216\n\t"
        "v_cvt_pk_bf16_f32 v19, v217, v33\n\t"
        "v_smfmac_f32_32x32x32_bf16 v[68:83], v[188:191], v[116:123], v223\n\t"
        "s_waitcnt lgkmcnt(4)\n\t"
        "ds_read_b64_tr_b16 v[184:185], v244 offset:896\n\t"
        "ds_write_b128 v232, v[16:19] offset:44032\n\t"
        "s_setprio 0\n\t"
        "v_exp_f32 v128, v128\n\t"
        "v_exp_f32 v129, v129\n\t"
        "v_exp_f32 v130, v130\n\t"
        "v_exp_f32 v131, v131\n\t"
        "v_mfma_f32_32x32x16_bf16 v[148:163], v[176:179], v[24:27], 0\n\t"
        "s_waitcnt lgkmcnt(5)\n\t"
        "ds_read_b64_tr_b16 v[186:187], v244 offset:1120\n\t"
        "ds_read_b64_tr_b16 v[188:189], v245 offset:0\n\t"
        "v_exp_f32 v132, v132\n\t"
        "v_exp_f32 v133, v133\n\t"
        "v_exp_f32 v134, v134\n\t"
        "v_exp_f32 v135, v135\n\t"
        "v_mfma_f32_32x32x16_bf16 v[148:163], v[172:175], v[20:23], v[148:163]\n\t"
        "ds_read_b64_tr_b16 v[190:191], v245 offset:224\n\t"
        "v_exp_f32 v136, v136\n\t"
        "v_exp_f32 v137, v137\n\t"
        "v_exp_f32 v138, v138\n\t"
        "v_exp_f32 v139, v139\n\t"
        "v_smfmac_f32_32x32x32_bf16 v[68:83], v[192:195], v[124:131], v223\n\t"
        "s_waitcnt lgkmcnt(6)\n\t"
        "v_exp_f32 v140, v140\n\t"
        "v_exp_f32 v141, v141\n\t"
        "v_exp_f32 v142, v142\n\t"
        "v_exp_f32 v143, v143\n\t"
        "v_smfmac_f32_32x32x32_bf16 v[68:83], v[180:183], v[132:139], v223\n\t"
        "ds_read_b64_tr_b16 v[192:193], v245 offset:896\n\t"
        "v_exp_f32 v144, v144\n\t"
        "v_exp_f32 v145, v145\n\t"
        "v_exp_f32 v146, v146\n\t"
        "v_exp_f32 v147, v147\n\t"
        "v_mfma_f32_32x32x16_bf16 v[100:115], v[168:171], v[16:19], 0\n\t"
        "s_waitcnt lgkmcnt(0)\n\t"
        "s_barrier\n\t"
        "ds_read_b128 v[32:35], v233 offset:44032\n\t"
        "ds_read_b128 v[28:31], v233 offset:43008\n\t"
        "ds_read_b64_tr_b16 v[194:195], v245 offset:1120\n\t"
        "s_setprio 2\n\t"
        "v_exp_f32 v148, v148\n\t"
        "v_exp_f32 v149, v149\n\t"
        "v_exp_f32 v150, v150\n\t"
        "v_exp_f32 v151, v151\n\t"
        "v_mfma_f32_32x32x16_bf16 v[100:115], v[164:167], v[12:15], v[100:115]\n\t"
        "v_exp_f32 v152, v152\n\t"
        "v_exp_f32 v153, v153\n\t"
        "v_exp_f32 v154, v154\n\t"
        "v_exp_f32 v155, v155\n\t"
        "v_smfmac_f32_32x32x32_bf16 v[68:83], v[184:187], v[140:147], v223\n\t"
        "s_add_u32 s50, s50, s47\n\t"
        "s_addc_u32 s51, s51, 0\n\t"
        "v_exp_f32 v156, v156\n\t"
        "v_exp_f32 v157, v157\n\t"
        "v_exp_f32 v158, v158\n\t"
        "v_exp_f32 v159, v159\n\t"
        "v_smfmac_f32_32x32x32_bf16 v[68:83], v[188:191], v[148:155], v223\n\t"
        "s_waitcnt lgkmcnt(2)\n\t"
        "s_add_u32 s52, s52, s47\n\t"
        "s_addc_u32 s53, s53, 0\n\t"
        "v_exp_f32 v160, v160\n\t"
        "v_exp_f32 v161, v161\n\t"
        "v_exp_f32 v162, v162\n\t"
        "v_exp_f32 v163, v163\n\t"
        "v_mfma_f32_32x32x16_bf16 v[116:131], v[168:171], v[32:35], 0\n\t"
        "s_waitcnt lgkmcnt(1)\n\t"
        "s_add_u32 s45, s45, s47\n\t"
        "v_exp_f32 v100, v100\n\t"
        "v_exp_f32 v101, v101\n\t"
        "v_exp_f32 v102, v102\n\t"
        "v_exp_f32 v103, v103\n\t"
        "v_mfma_f32_32x32x16_bf16 v[116:131], v[164:167], v[28:31], v[116:131]\n\t"
        "s_waitcnt lgkmcnt(0)\n\t"
        "v_exp_f32 v104, v104\n\t"
        "v_exp_f32 v105, v105\n\t"
        "v_exp_f32 v106, v106\n\t"
        "v_exp_f32 v107, v107\n\t"
        "v_smfmac_f32_32x32x32_bf16 v[68:83], v[192:195], v[156:163], v223\n\t"
        "v_exp_f32 v108, v108\n\t"
        "v_exp_f32 v109, v109\n\t"
        "v_exp_f32 v110, v110\n\t"
        "v_exp_f32 v111, v111\n\t"
        "v_smfmac_f32_32x32x32_bf16 v[52:67], v[180:183], v[100:107], v223\n\t"
        "v_exp_f32 v112, v112\n\t"
        "v_exp_f32 v113, v113\n\t"
        "v_exp_f32 v114, v114\n\t"
        "v_exp_f32 v115, v115\n\t"
        "v_mfma_f32_32x32x16_bf16 v[132:147], v[176:179], v[16:19], 0\n\t"
        "ds_read_b128 v[168:171], v242 offset:25600\n\t"
        "v_exp_f32 v116, v116\n\t"
        "v_exp_f32 v117, v117\n\t"
        "v_exp_f32 v118, v118\n\t"
        "v_exp_f32 v119, v119\n\t"
        "v_mfma_f32_32x32x16_bf16 v[132:147], v[172:175], v[12:15], v[132:147]\n\t"
        "ds_read_b128 v[164:167], v242 offset:24576\n\t"
        "v_exp_f32 v120, v120\n\t"
        "v_exp_f32 v121, v121\n\t"
        "v_exp_f32 v122, v122\n\t"
        "v_exp_f32 v123, v123\n\t"
        "v_smfmac_f32_32x32x32_bf16 v[52:67], v[184:187], v[108:115], v223\n\t"
        "s_waitcnt vmcnt(0)\n\t"
        "s_setprio 3\n\t"
        "v_exp_f32 v124, v124\n\t"
        "v_exp_f32 v125, v125\n\t"
        "v_exp_f32 v126, v126\n\t"
        "v_exp_f32 v127, v127\n\t"
        "v_dot2_f32_bf16 v2, v196, v196, 0\n\t"
        "v_dot2_f32_bf16 v3, v197, v197, 0\n\t"
        "v_dot2_f32_bf16 v2, v198, v198, v2\n\t"
        "v_dot2_f32_bf16 v3, v199, v199, v3\n\t"
        "v_smfmac_f32_32x32x32_bf16 v[84:99], v[180:183], v[116:123], v223\n\t"
        "v_exp_f32 v128, v128\n\t"
        "v_exp_f32 v129, v129\n\t"
        "v_exp_f32 v130, v130\n\t"
        "v_exp_f32 v131, v131\n\t"
        "v_dot2_f32_bf16 v2, v200, v200, v2\n\t"
        "v_dot2_f32_bf16 v3, v201, v201, v3\n\t"
        "v_dot2_f32_bf16 v2, v202, v202, v2\n\t"
        "v_mfma_f32_32x32x16_bf16 v[148:163], v[176:179], v[32:35], 0\n\t"
        "ds_read_b64_tr_b16 v[180:181], v246 offset:0\n\t"
        "s_setprio 2\n\t"
        "v_exp_f32 v132, v132\n\t"
        "v_exp_f32 v133, v133\n\t"
        "v_exp_f32 v134, v134\n\t"
        "v_exp_f32 v135, v135\n\t"
        "v_add_f32 v2, v2, v3\n\t"
        "v_mfma_f32_32x32x16_bf16 v[148:163], v[172:175], v[28:31], v[148:163]\n\t"
        "ds_read_b64_tr_b16 v[182:183], v246 offset:224\n\t"
        "v_exp_f32 v136, v136\n\t"
        "v_exp_f32 v137, v137\n\t"
        "v_exp_f32 v138, v138\n\t"
        "v_mfma_f32_32x32x16_bf16 v[204:219], v[248:251], v[196:199], 0\n\t"
        "v_exp_f32 v139, v139\n\t"
        "v_add_f32_dpp v2, v2, v2 quad_perm:[1,0,3,2] row_mask:0xf bank_mask:0xf bound_ctrl:1\n\t"
        "v_smfmac_f32_32x32x32_bf16 v[84:99], v[184:187], v[124:131], v223\n\t"
        "v_exp_f32 v140, v140\n\t"
        "v_exp_f32 v141, v141\n\t"
        "v_exp_f32 v142, v142\n\t"
        "v_exp_f32 v143, v143\n\t"
        "v_sqrt_f32 v2, v2\n\t"
        "v_smfmac_f32_32x32x32_bf16 v[52:67], v[188:191], v[132:139], v223\n\t"
        "ds_read_b64_tr_b16 v[184:185], v246 offset:896\n\t"
        "v_exp_f32 v144, v144\n\t"
        "v_exp_f32 v145, v145\n\t"
        "v_exp_f32 v146, v146\n\t"
        "v_mfma_f32_32x32x16_bf16 v[204:219], v[252:255], v[200:203], v[204:219]\n\t"
        "s_waitcnt lgkmcnt(4)\n\t"
        "v_exp_f32 v147, v147\n\t"
        "v_add_f32 v2, s68, v2\n\t"
        "v_mfma_f32_32x32x16_bf16 v[100:115], v[168:171], v[16:19], 0\n\t"
        "s_waitcnt lgkmcnt(3)\n\t"
        "ds_read_b64_tr_b16 v[186:187], v246 offset:1120\n\t"
        "ds_read_b128 v[176:179], v243 offset:25600\n\t"
        "s_branch 13f\n\t"
        ".p2align 12\n\t"
        "13:\n\t"
        "v_exp_f32 v148, v148\n\t"
        "v_exp_f32 v149, v149\n\t"
        "v_exp_f32 v150, v150\n\t"
        "v_exp_f32 v151, v151\n\t"
        "v_rcp_f32 v2, v2\n\t"
        "v_mfma_f32_32x32x16_bf16 v[100:115], v[164:167], v[12:15], v[100:115]\n\t"
        "ds_read_b128 v[172:175], v243 offset:24576\n\t"
        "v_exp_f32 v152, v152\n\t"
        "v_exp_f32 v153, v153\n\t"
        "v_exp_f32 v154, v154\n\t"
        "v_exp_f32 v155, v155\n\t"
        "v_smfmac_f32_32x32x32_bf16 v[52:67], v[192:195], v[140:147], v223\n\t"
        "s_mov_b32 m0, s45\n\t"
        "global_load_dwordx4 v[196:199], v224, s[50:51] offset:0\n\t"
        "global_load_lds_dwordx4 v225, s[52:53]\n\t"
        "global_load_lds_dwordx4 v225, s[52:53] offset:1024\n\t"
        "global_load_dwordx3 v[200:202], v224, s[50:51] offset:16\n\t"
        "s_setprio 3\n\t"
        "v_exp_f32 v156, v156\n\t"
        "v_exp_f32 v157, v157\n\t"
        "v_exp_f32 v158, v158\n\t"
        "v_exp_f32 v159, v159\n\t"
        "v_pk_mul_f32 v[204:205], v[2:3], v[204:205] op_sel_hi:[0,1]\n\t"
        "v_pk_mul_f32 v[206:207], v[2:3], v[206:207] op_sel_hi:[0,1]\n\t"
        "v_pk_mul_f32 v[208:209], v[2:3], v[208:209] op_sel_hi:[0,1]\n\t"
        "v_pk_mul_f32 v[210:211], v[2:3], v[210:211] op_sel_hi:[0,1]\n\t"
        "v_smfmac_f32_32x32x32_bf16 v[84:99], v[188:191], v[148:155], v223\n\t"
        "v_exp_f32 v160, v160\n\t"
        "v_exp_f32 v161, v161\n\t"
        "v_exp_f32 v162, v162\n\t"
        "v_exp_f32 v163, v163\n\t"
        "v_pk_mul_f32 v[212:213], v[2:3], v[212:213] op_sel_hi:[0,1]\n\t"
        "v_pk_mul_f32 v[214:215], v[2:3], v[214:215] op_sel_hi:[0,1]\n\t"
        "v_pk_mul_f32 v[216:217], v[2:3], v[216:217] op_sel_hi:[0,1]\n\t"
        "v_mfma_f32_32x32x16_bf16 v[116:131], v[168:171], v[32:35], 0\n\t"
        "ds_read_b64_tr_b16 v[188:189], v247 offset:0\n\t"
        "s_setprio 1\n\t"
        "v_exp_f32 v100, v100\n\t"
        "v_exp_f32 v101, v101\n\t"
        "v_exp_f32 v102, v102\n\t"
        "v_exp_f32 v103, v103\n\t"
        "v_cvt_pk_bf16_f32 v204, v204, v205\n\t"
        "v_cvt_pk_bf16_f32 v205, v206, v207\n\t"
        "v_mfma_f32_32x32x16_bf16 v[116:131], v[164:167], v[28:31], v[116:131]\n\t"
        "ds_read_b64_tr_b16 v[190:191], v247 offset:224\n\t"
        "v_exp_f32 v104, v104\n\t"
        "v_exp_f32 v105, v105\n\t"
        "v_exp_f32 v106, v106\n\t"
        "v_exp_f32 v107, v107\n\t"
        "v_cvt_pk_bf16_f32 v206, v208, v209\n\t"
        "v_cvt_pk_bf16_f32 v207, v210, 1.0\n\t"
        "v_cvt_pk_bf16_f32 v208, v211, v212\n\t"
        "v_smfmac_f32_32x32x32_bf16 v[84:99], v[192:195], v[156:163], v223\n\t"
        "s_waitcnt lgkmcnt(6)\n\t"
        "ds_write_b128 v222, v[204:207] offset:8192\n\t"
        "v_exp_f32 v108, v108\n\t"
        "v_exp_f32 v109, v109\n\t"
        "v_exp_f32 v110, v110\n\t"
        "v_exp_f32 v111, v111\n\t"
        "v_cvt_pk_bf16_f32 v209, v213, v214\n\t"
        "v_cvt_pk_bf16_f32 v210, v215, v216\n\t"
        "v_cvt_pk_bf16_f32 v211, v217, 1.0\n\t"
        "v_smfmac_f32_32x32x32_bf16 v[52:67], v[180:183], v[100:107], v223\n\t"
        "s_waitcnt lgkmcnt(4)\n\t"
        "ds_read_b64_tr_b16 v[192:193], v247 offset:896\n\t"
        "ds_write_b128 v222, v[208:211] offset:9216\n\t"
        "v_exp_f32 v112, v112\n\t"
        "v_exp_f32 v113, v113\n\t"
        "v_exp_f32 v114, v114\n\t"
        "v_exp_f32 v115, v115\n\t"
        "v_mfma_f32_32x32x16_bf16 v[132:147], v[176:179], v[16:19], 0\n\t"
        "s_waitcnt lgkmcnt(5)\n\t"
        "ds_read_b64_tr_b16 v[194:195], v247 offset:1120\n\t"
        "s_setprio 0\n\t"
        "v_exp_f32 v116, v116\n\t"
        "v_exp_f32 v117, v117\n\t"
        "v_exp_f32 v118, v118\n\t"
        "v_exp_f32 v119, v119\n\t"
        "v_mfma_f32_32x32x16_bf16 v[132:147], v[172:175], v[12:15], v[132:147]\n\t"
        "v_exp_f32 v120, v120\n\t"
        "v_exp_f32 v121, v121\n\t"
        "v_exp_f32 v122, v122\n\t"
        "v_exp_f32 v123, v123\n\t"
        "v_smfmac_f32_32x32x32_bf16 v[52:67], v[184:187], v[108:115], v223\n\t"
        "s_waitcnt lgkmcnt(0)\n\t"
        "s_barrier\n\t"
        "ds_read_b128 v[168:171], v220 offset:9216\n\t"
        "v_exp_f32 v124, v124\n\t"
        "v_exp_f32 v125, v125\n\t"
        "v_exp_f32 v126, v126\n\t"
        "v_exp_f32 v127, v127\n\t"
        "v_smfmac_f32_32x32x32_bf16 v[84:99], v[180:183], v[116:123], v223\n\t"
        "ds_read_b128 v[164:167], v220 offset:8192\n\t"
        "s_setprio 2\n\t"
        "v_exp_f32 v128, v128\n\t"
        "v_exp_f32 v129, v129\n\t"
        "v_exp_f32 v130, v130\n\t"
        "v_exp_f32 v131, v131\n\t"
        "v_mfma_f32_32x32x16_bf16 v[148:163], v[176:179], v[32:35], 0\n\t"
        "ds_read_b64_tr_b16 v[180:181], v221 offset:0\n\t"
        "v_exp_f32 v132, v132\n\t"
        "v_exp_f32 v133, v133\n\t"
        "v_exp_f32 v134, v134\n\t"
        "v_exp_f32 v135, v135\n\t"
        "v_xor_b32 v222, 0x4000, v222\n\t"
        "v_mfma_f32_32x32x16_bf16 v[148:163], v[172:175], v[28:31], v[148:163]\n\t"
        "ds_read_b64_tr_b16 v[182:183], v221 offset:224\n\t"
        "v_exp_f32 v136, v136\n\t"
        "v_exp_f32 v137, v137\n\t"
        "v_exp_f32 v138, v138\n\t"
        "v_exp_f32 v139, v139\n\t"
        "v_smfmac_f32_32x32x32_bf16 v[84:99], v[184:187], v[124:131], v223\n\t"
        "s_add_u32 s50, s50, s47\n\t"
        "s_addc_u32 s51, s51, 0\n\t"
        "v_exp_f32 v140, v140\n\t"
        "v_exp_f32 v141, v141\n\t"
        "v_exp_f32 v142, v142\n\t"
        "v_exp_f32 v143, v143\n\t"
        "v_smfmac_f32_32x32x32_bf16 v[52:67], v[188:191], v[132:139], v223\n\t"
        "s_waitcnt lgkmcnt(3)\n\t"
        "ds_read_b64_tr_b16 v[184:185], v221 offset:896\n\t"
        "v_exp_f32 v144, v144\n\t"
        "v_exp_f32 v145, v145\n\t"
        "v_exp_f32 v146, v146\n\t"
        "v_exp_f32 v147, v147\n\t"
        "v_mfma_f32_32x32x16_bf16 v[100:115], v[168:171], v[8:11], 0\n\t"
        "s_waitcnt lgkmcnt(3)\n\t"
        "ds_read_b64_tr_b16 v[186:187], v221 offset:1120\n\t"
        "v_exp_f32 v148, v148\n\t"
        "v_exp_f32 v149, v149\n\t"
        "v_exp_f32 v150, v150\n\t"
        "v_exp_f32 v151, v151\n\t"
        "v_mfma_f32_32x32x16_bf16 v[100:115], v[164:167], v[4:7], v[100:115]\n\t"
        "s_add_u32 s52, s52, s47\n\t"
        "s_addc_u32 s53, s53, 0\n\t"
        "v_exp_f32 v152, v152\n\t"
        "v_exp_f32 v153, v153\n\t"
        "v_exp_f32 v154, v154\n\t"
        "v_exp_f32 v155, v155\n\t"
        "v_smfmac_f32_32x32x32_bf16 v[52:67], v[192:195], v[140:147], v223\n\t"
        "s_add_u32 s45, s45, s47\n\t"
        "v_exp_f32 v156, v156\n\t"
        "v_exp_f32 v157, v157\n\t"
        "v_exp_f32 v158, v158\n\t"
        "v_exp_f32 v159, v159\n\t"
        "v_smfmac_f32_32x32x32_bf16 v[84:99], v[188:191], v[148:155], v223\n\t"
        "s_waitcnt lgkmcnt(0)\n\t"
        "v_exp_f32 v160, v160\n\t"
        "v_exp_f32 v161, v161\n\t"
        "v_exp_f32 v162, v162\n\t"
        "v_exp_f32 v163, v163\n\t"
        "1:\n\t"
        "v_mfma_f32_32x32x16_bf16 v[116:131], v[168:171], v[16:19], 0\n\t"
        "ds_read_b128 v[176:179], v220 offset:11264\n\t"
        "v_exp_f32 v100, v100\n\t"
        "v_exp_f32 v101, v101\n\t"
        "v_exp_f32 v102, v102\n\t"
        "v_exp_f32 v103, v103\n\t"
        "v_mfma_f32_32x32x16_bf16 v[116:131], v[164:167], v[12:15], v[116:131]\n\t"
        "ds_read_b128 v[172:175], v220 offset:10240\n\t"
        "v_exp_f32 v104, v104\n\t"
        "v_exp_f32 v105, v105\n\t"
        "v_exp_f32 v106, v106\n\t"
        "v_exp_f32 v107, v107\n\t"
        "v_smfmac_f32_32x32x32_bf16 v[84:99], v[192:195], v[156:163], v223\n\t"
        "v_exp_f32 v108, v108\n\t"
        "v_exp_f32 v109, v109\n\t"
        "v_exp_f32 v110, v110\n\t"
        "v_exp_f32 v111, v111\n\t"
        "v_smfmac_f32_32x32x32_bf16 v[36:51], v[180:183], v[100:107], v223\n\t"
        "ds_read_b64_tr_b16 v[188:189], v221 offset:1792\n\t"
        "v_exp_f32 v112, v112\n\t"
        "v_exp_f32 v113, v113\n\t"
        "v_exp_f32 v114, v114\n\t"
        "v_exp_f32 v115, v115\n\t"
        "v_mfma_f32_32x32x16_bf16 v[132:147], v[168:171], v[24:27], 0\n\t"
        "ds_read_b64_tr_b16 v[190:191], v221 offset:2016\n\t"
        "v_exp_f32 v116, v116\n\t"
        "v_exp_f32 v117, v117\n\t"
        "v_exp_f32 v118, v118\n\t"
        "v_exp_f32 v119, v119\n\t"
        "v_mfma_f32_32x32x16_bf16 v[132:147], v[164:167], v[20:23], v[132:147]\n\t"
        "ds_read_b64_tr_b16 v[192:193], v221 offset:2688\n\t"
        "v_exp_f32 v120, v120\n\t"
        "v_exp_f32 v121, v121\n\t"
        "v_exp_f32 v122, v122\n\t"
        "v_exp_f32 v123, v123\n\t"
        "v_smfmac_f32_32x32x32_bf16 v[36:51], v[184:187], v[108:115], v223\n\t"
        "ds_read_b64_tr_b16 v[194:195], v221 offset:2912\n\t"
        "v_exp_f32 v124, v124\n\t"
        "v_exp_f32 v125, v125\n\t"
        "v_exp_f32 v126, v126\n\t"
        "v_exp_f32 v127, v127\n\t"
        "v_smfmac_f32_32x32x32_bf16 v[52:67], v[180:183], v[116:123], v223\n\t"
        "v_exp_f32 v128, v128\n\t"
        "v_exp_f32 v129, v129\n\t"
        "v_exp_f32 v130, v130\n\t"
        "v_exp_f32 v131, v131\n\t"
        "v_mfma_f32_32x32x16_bf16 v[148:163], v[168:171], v[32:35], 0\n\t"
        "v_exp_f32 v132, v132\n\t"
        "v_exp_f32 v133, v133\n\t"
        "v_exp_f32 v134, v134\n\t"
        "v_exp_f32 v135, v135\n\t"
        "v_mfma_f32_32x32x16_bf16 v[148:163], v[164:167], v[28:31], v[148:163]\n\t"
        "v_exp_f32 v136, v136\n\t"
        "v_exp_f32 v137, v137\n\t"
        "v_exp_f32 v138, v138\n\t"
        "v_exp_f32 v139, v139\n\t"
        "v_smfmac_f32_32x32x32_bf16 v[52:67], v[184:187], v[124:131], v223\n\t"
        "v_exp_f32 v140, v140\n\t"
        "v_exp_f32 v141, v141\n\t"
        "v_exp_f32 v142, v142\n\t"
        "v_exp_f32 v143, v143\n\t"
        "v_smfmac_f32_32x32x32_bf16 v[68:83], v[180:183], v[132:139], v223\n\t"
        "s_waitcnt lgkmcnt(0)\n\t"
        "v_exp_f32 v144, v144\n\t"
        "v_exp_f32 v145, v145\n\t"
        "v_exp_f32 v146, v146\n\t"
        "v_exp_f32 v147, v147\n\t"
        "v_mfma_f32_32x32x16_bf16 v[100:115], v[176:179], v[8:11], 0\n\t"
        "v_exp_f32 v148, v148\n\t"
        "v_exp_f32 v149, v149\n\t"
        "v_exp_f32 v150, v150\n\t"
        "v_exp_f32 v151, v151\n\t"
        "v_mfma_f32_32x32x16_bf16 v[100:115], v[172:175], v[4:7], v[100:115]\n\t"
        "v_exp_f32 v152, v152\n\t"
        "v_exp_f32 v153, v153\n\t"
        "v_exp_f32 v154, v154\n\t"
        "v_exp_f32 v155, v155\n\t"
        "v_smfmac_f32_32x32x32_bf16 v[68:83], v[184:187], v[140:147], v223\n\t"
        "v_exp_f32 v156, v156\n\t"
        "v_exp_f32 v157, v157\n\t"
        "v_exp_f32 v158, v158\n\t"
        "v_exp_f32 v159, v159\n\t"
        "v_smfmac_f32_32x32x32_bf16 v[84:99], v[180:183], v[148:155], v223\n\t"
        "v_exp_f32 v160, v160\n\t"
        "v_exp_f32 v161, v161\n\t"
        "v_exp_f32 v162, v162\n\t"
        "v_exp_f32 v163, v163\n\t"
        "v_mfma_f32_32x32x16_bf16 v[116:131], v[176:179], v[16:19], 0\n\t"
        "ds_read_b128 v[168:171], v220 offset:13312\n\t"
        "v_exp_f32 v100, v100\n\t"
        "v_exp_f32 v101, v101\n\t"
        "v_exp_f32 v102, v102\n\t"
        "v_exp_f32 v103, v103\n\t"
        "v_mfma_f32_32x32x16_bf16 v[116:131], v[172:175], v[12:15], v[116:131]\n\t"
        "ds_read_b128 v[164:167], v220 offset:12288\n\t"
        "v_exp_f32 v104, v104\n\t"
        "v_exp_f32 v105, v105\n\t"
        "v_exp_f32 v106, v106\n\t"
        "v_exp_f32 v107, v107\n\t"
        "v_smfmac_f32_32x32x32_bf16 v[84:99], v[184:187], v[156:163], v223\n\t"
        "s_waitcnt vmcnt(0)\n\t"
        "s_setprio 3\n\t"
        "v_exp_f32 v108, v108\n\t"
        "v_exp_f32 v109, v109\n\t"
        "v_exp_f32 v110, v110\n\t"
        "v_exp_f32 v111, v111\n\t"
        "v_dot2_f32_bf16 v2, v196, v196, 0\n\t"
        "v_dot2_f32_bf16 v3, v197, v197, 0\n\t"
        "v_dot2_f32_bf16 v2, v198, v198, v2\n\t"
        "v_dot2_f32_bf16 v3, v199, v199, v3\n\t"
        "v_smfmac_f32_32x32x32_bf16 v[36:51], v[188:191], v[100:107], v223\n\t"
        "ds_read_b64_tr_b16 v[180:181], v221 offset:3584\n\t"
        "v_exp_f32 v112, v112\n\t"
        "v_exp_f32 v113, v113\n\t"
        "v_exp_f32 v114, v114\n\t"
        "v_exp_f32 v115, v115\n\t"
        "v_dot2_f32_bf16 v2, v200, v200, v2\n\t"
        "v_dot2_f32_bf16 v3, v201, v201, v3\n\t"
        "v_dot2_f32_bf16 v2, v202, v202, v2\n\t"
        "v_mfma_f32_32x32x16_bf16 v[132:147], v[176:179], v[24:27], 0\n\t"
        "ds_read_b64_tr_b16 v[182:183], v221 offset:3808\n\t"
        "s_setprio 2\n\t"
        "v_exp_f32 v116, v116\n\t"
        "v_exp_f32 v117, v117\n\t"
        "v_exp_f32 v118, v118\n\t"
        "v_exp_f32 v119, v119\n\t"
        "v_add_f32 v2, v2, v3\n\t"
        "v_mfma_f32_32x32x16_bf16 v[132:147], v[172:175], v[20:23], v[132:147]\n\t"
        "ds_read_b64_tr_b16 v[184:185], v221 offset:4480\n\t"
        "v_exp_f32 v120, v120\n\t"
        "v_exp_f32 v121, v121\n\t"
        "v_exp_f32 v122, v122\n\t"
        "v_mfma_f32_32x32x16_bf16 v[204:219], v[248:251], v[196:199], 0\n\t"
        "v_exp_f32 v123, v123\n\t"
        "v_add_f32_dpp v2, v2, v2 quad_perm:[1,0,3,2] row_mask:0xf bank_mask:0xf bound_ctrl:1\n\t"
        "v_smfmac_f32_32x32x32_bf16 v[36:51], v[192:195], v[108:115], v223\n\t"
        "ds_read_b64_tr_b16 v[186:187], v221 offset:4704\n\t"
        "v_exp_f32 v124, v124\n\t"
        "v_exp_f32 v125, v125\n\t"
        "v_exp_f32 v126, v126\n\t"
        "v_exp_f32 v127, v127\n\t"
        "v_sqrt_f32 v2, v2\n\t"
        "v_smfmac_f32_32x32x32_bf16 v[52:67], v[188:191], v[116:123], v223\n\t"
        "v_exp_f32 v128, v128\n\t"
        "v_exp_f32 v129, v129\n\t"
        "v_exp_f32 v130, v130\n\t"
        "v_mfma_f32_32x32x16_bf16 v[204:219], v[252:255], v[200:203], v[204:219]\n\t"
        "v_exp_f32 v131, v131\n\t"
        "v_add_f32 v2, s68, v2\n\t"
        "v_mfma_f32_32x32x16_bf16 v[148:163], v[176:179], v[32:35], 0\n\t"
        "v_exp_f32 v132, v132\n\t"
        "v_exp_f32 v133, v133\n\t"
        "v_exp_f32 v134, v134\n\t"
        "v_exp_f32 v135, v135\n\t"
        "v_rcp_f32 v2, v2\n\t"
        "v_mfma_f32_32x32x16_bf16 v[148:163], v[172:175], v[28:31], v[148:163]\n\t"
        "s_cmp_lt_u32 s44, 6\n\t"
        "v_exp_f32 v136, v136\n\t"
        "v_exp_f32 v137, v137\n\t"
        "v_exp_f32 v138, v138\n\t"
        "v_exp_f32 v139, v139\n\t"
        "v_smfmac_f32_32x32x32_bf16 v[52:67], v[192:195], v[124:131], v223\n\t"
        "s_cbranch_scc0 3f\n\t"
        "s_mov_b32 m0, s45\n\t"
        "global_load_dwordx4 v[196:199], v224, s[50:51] offset:0\n\t"
        "global_load_lds_dwordx4 v225, s[52:53]\n\t"
        "global_load_lds_dwordx4 v225, s[52:53] offset:1024\n\t"
        "global_load_dwordx3 v[200:202], v224, s[50:51] offset:16\n\t"
        "3:\n\t"
        "s_setprio 3\n\t"
        "v_exp_f32 v140, v140\n\t"
        "v_exp_f32 v141, v141\n\t"
        "v_exp_f32 v142, v142\n\t"
        "v_exp_f32 v143, v143\n\t"
        "v_pk_mul_f32 v[204:205], v[2:3], v[204:205] op_sel_hi:[0,1]\n\t"
        "v_pk_mul_f32 v[206:207], v[2:3], v[206:207] op_sel_hi:[0,1]\n\t"
        "v_pk_mul_f32 v[208:209], v[2:3], v[208:209] op_sel_hi:[0,1]\n\t"
        "v_pk_mul_f32 v[210:211], v[2:3], v[210:211] op_sel_hi:[0,1]\n\t"
        "v_smfmac_f32_32x32x32_bf16 v[68:83], v[188:191], v[132:139], v223\n\t"
        "s_waitcnt lgkmcnt(0)\n\t"
        "v_exp_f32 v144, v144\n\t"
        "v_exp_f32 v145, v145\n\t"
        "v_exp_f32 v146, v146\n\t"
        "v_exp_f32 v147, v147\n\t"
        "v_pk_mul_f32 v[212:213], v[2:3], v[212:213] op_sel_hi:[0,1]\n\t"
        "v_pk_mul_f32 v[214:215], v[2:3], v[214:215] op_sel_hi:[0,1]\n\t"
        "v_pk_mul_f32 v[216:217], v[2:3], v[216:217] op_sel_hi:[0,1]\n\t"
        "v_mfma_f32_32x32x16_bf16 v[100:115], v[168:171], v[8:11], 0\n\t"
        "s_setprio 2\n\t"
        "v_exp_f32 v148, v148\n\t"
        "v_exp_f32 v149, v149\n\t"
        "v_exp_f32 v150, v150\n\t"
        "v_exp_f32 v151, v151\n\t"
        "v_cvt_pk_bf16_f32 v204, v204, v205\n\t"
        "v_cvt_pk_bf16_f32 v205, v206, v207\n\t"
        "v_mfma_f32_32x32x16_bf16 v[100:115], v[164:167], v[4:7], v[100:115]\n\t"
        "v_exp_f32 v152, v152\n\t"
        "v_exp_f32 v153, v153\n\t"
        "v_exp_f32 v154, v154\n\t"
        "v_exp_f32 v155, v155\n\t"
        "v_cvt_pk_bf16_f32 v206, v208, v209\n\t"
        "v_cvt_pk_bf16_f32 v207, v210, 1.0\n\t"
        "v_cvt_pk_bf16_f32 v208, v211, v212\n\t"
        "v_smfmac_f32_32x32x32_bf16 v[68:83], v[192:195], v[140:147], v223\n\t"
        "ds_write_b128 v222, v[204:207] offset:8192\n\t"
        "v_exp_f32 v156, v156\n\t"
        "v_exp_f32 v157, v157\n\t"
        "v_exp_f32 v158, v158\n\t"
        "v_exp_f32 v159, v159\n\t"
        "v_cvt_pk_bf16_f32 v209, v213, v214\n\t"
        "v_cvt_pk_bf16_f32 v210, v215, v216\n\t"
        "v_cvt_pk_bf16_f32 v211, v217, 1.0\n\t"
        "v_smfmac_f32_32x32x32_bf16 v[84:99], v[188:191], v[148:155], v223\n\t"
        "ds_write_b128 v222, v[208:211] offset:9216\n\t"
        "v_exp_f32 v160, v160\n\t"
        "v_exp_f32 v161, v161\n\t"
        "v_exp_f32 v162, v162\n\t"
        "v_exp_f32 v163, v163\n\t"
        "v_mfma_f32_32x32x16_bf16 v[116:131], v[168:171], v[16:19], 0\n\t"
        "ds_read_b128 v[176:179], v220 offset:15360\n\t"
        "v_exp_f32 v100, v100\n\t"
        "v_exp_f32 v101, v101\n\t"
        "v_exp_f32 v102, v102\n\t"
        "v_exp_f32 v103, v103\n\t"
        "v_mfma_f32_32x32x16_bf16 v[116:131], v[164:167], v[12:15], v[116:131]\n\t"
        "ds_read_b128 v[172:175], v220 offset:14336\n\t"
        "s_setprio 1\n\t"
        "v_exp_f32 v104, v104\n\t"
        "v_exp_f32 v105, v105\n\t"
        "v_exp_f32 v106, v106\n\t"
        "v_exp_f32 v107, v107\n\t"
        "v_smfmac_f32_32x32x32_bf16 v[84:99], v[192:195], v[156:163], v223\n\t"
        "v_exp_f32 v108, v108\n\t"
        "v_exp_f32 v109, v109\n\t"
        "v_exp_f32 v110, v110\n\t"
        "v_exp_f32 v111, v111\n\t"
        "v_smfmac_f32_32x32x32_bf16 v[36:51], v[180:183], v[100:107], v223\n\t"
        "ds_read_b64_tr_b16 v[188:189], v221 offset:5376\n\t"
        "v_exp_f32 v112, v112\n\t"
        "v_exp_f32 v113, v113\n\t"
        "v_exp_f32 v114, v114\n\t"
        "v_exp_f32 v115, v115\n\t"
        "v_mfma_f32_32x32x16_bf16 v[132:147], v[168:171], v[24:27], 0\n\t"
        "ds_read_b64_tr_b16 v[190:191], v221 offset:5600\n\t"
        "v_exp_f32 v116, v116\n\t"
        "v_exp_f32 v117, v117\n\t"
        "v_exp_f32 v118, v118\n\t"
        "v_exp_f32 v119, v119\n\t"
        "v_mfma_f32_32x32x16_bf16 v[132:147], v[164:167], v[20:23], v[132:147]\n\t"
        "ds_read_b64_tr_b16 v[192:193], v221 offset:6272\n\t"
        "v_exp_f32 v120, v120\n\t"
        "v_exp_f32 v121, v121\n\t"
        "v_exp_f32 v122, v122\n\t"
        "v_exp_f32 v123, v123\n\t"
        "v_smfmac_f32_32x32x32_bf16 v[36:51], v[184:187], v[108:115], v223\n\t"
        "ds_read_b64_tr_b16 v[194:195], v221 offset:6496\n\t"
        "v_exp_f32 v124, v124\n\t"
        "v_exp_f32 v125, v125\n\t"
        "v_exp_f32 v126, v126\n\t"
        "v_exp_f32 v127, v127\n\t"
        "v_smfmac_f32_32x32x32_bf16 v[52:67], v[180:183], v[116:123], v223\n\t"
        "v_exp_f32 v128, v128\n\t"
        "v_exp_f32 v129, v129\n\t"
        "v_exp_f32 v130, v130\n\t"
        "v_exp_f32 v131, v131\n\t"
        "v_mfma_f32_32x32x16_bf16 v[148:163], v[168:171], v[32:35], 0\n\t"
        "v_exp_f32 v132, v132\n\t"
        "v_exp_f32 v133, v133\n\t"
        "v_exp_f32 v134, v134\n\t"
        "v_exp_f32 v135, v135\n\t"
        "v_xor_b32 v220, 0x4000, v220\n\t"
        "v_mfma_f32_32x32x16_bf16 v[148:163], v[164:167], v[28:31], v[148:163]\n\t"
        "s_setprio 0\n\t"
        "v_exp_f32 v136, v136\n\t"
        "v_exp_f32 v137, v137\n\t"
        "v_exp_f32 v138, v138\n\t"
        "v_exp_f32 v139, v139\n\t"
        "v_mad_u32_u24 v221, v227, s46, v226\n\t"
        "v_smfmac_f32_32x32x32_bf16 v[52:67], v[184:187], v[124:131], v223\n\t"
        "v_exp_f32 v140, v140\n\t"
        "v_exp_f32 v141, v141\n\t"
        "v_exp_f32 v142, v142\n\t"
        "v_exp_f32 v143, v143\n\t"
        "v_smfmac_f32_32x32x32_bf16 v[68:83], v[180:183], v[132:139], v223\n\t"
        "s_waitcnt lgkmcnt(0)\n\t"
        "s_barrier\n\t"
        "v_exp_f32 v144, v144\n\t"
        "v_exp_f32 v145, v145\n\t"
        "v_exp_f32 v146, v146\n\t"
        "v_exp_f32 v147, v147\n\t"
        "v_mfma_f32_32x32x16_bf16 v[100:115], v[176:179], v[8:11], 0\n\t"
        "s_setprio 2\n\t"
        "v_exp_f32 v148, v148\n\t"
        "v_exp_f32 v149, v149\n\t"
        "v_exp_f32 v150, v150\n\t"
        "v_exp_f32 v151, v151\n\t"
        "v_mfma_f32_32x32x16_bf16 v[100:115], v[172:175], v[4:7], v[100:115]\n\t"
        "v_exp_f32 v152, v152\n\t"
        "v_exp_f32 v153, v153\n\t"
        "v_exp_f32 v154, v154\n\t"
        "v_exp_f32 v155, v155\n\t"
        "v_smfmac_f32_32x32x32_bf16 v[68:83], v[184:187], v[140:147], v223\n\t"
        "v_exp_f32 v156, v156\n\t"
        "v_exp_f32 v157, v157\n\t"
        "v_exp_f32 v158, v158\n\t"
        "v_exp_f32 v159, v159\n\t"
        "v_smfmac_f32_32x32x32_bf16 v[84:99], v[180:183], v[148:155], v223\n\t"
        "v_exp_f32 v160, v160\n\t"
        "v_exp_f32 v161, v161\n\t"
        "v_exp_f32 v162, v162\n\t"
        "v_exp_f32 v163, v163\n\t"
        "v_mfma_f32_32x32x16_bf16 v[116:131], v[176:179], v[16:19], 0\n\t"
        "ds_read_b128 v[168:171], v220 offset:9216\n\t"
        "v_exp_f32 v100, v100\n\t"
        "v_exp_f32 v101, v101\n\t"
        "v_exp_f32 v102, v102\n\t"
        "v_exp_f32 v103, v103\n\t"
        "v_mfma_f32_32x32x16_bf16 v[116:131], v[172:175], v[12:15], v[116:131]\n\t"
        "ds_read_b128 v[164:167], v220 offset:8192\n\t"
        "v_exp_f32 v104, v104\n\t"
        "v_exp_f32 v105, v105\n\t"
        "v_exp_f32 v106, v106\n\t"
        "v_exp_f32 v107, v107\n\t"
        "v_smfmac_f32_32x32x32_bf16 v[84:99], v[184:187], v[156:163], v223\n\t"
        "v_exp_f32 v108, v108\n\t"
        "v_exp_f32 v109, v109\n\t"
        "v_exp_f32 v110, v110\n\t"
        "v_exp_f32 v111, v111\n\t"
        "v_smfmac_f32_32x32x32_bf16 v[36:51], v[188:191], v[100:107], v223\n\t"
        "ds_read_b64_tr_b16 v[180:181], v221 offset:0\n\t"
        "v_exp_f32 v112, v112\n\t"
        "v_exp_f32 v113, v113\n\t"
        "v_exp_f32 v114, v114\n\t"
        "v_exp_f32 v115, v115\n\t"
        "v_mfma_f32_32x32x16_bf16 v[132:147], v[176:179], v[24:27], 0\n\t"
        "ds_read_b64_tr_b16 v[182:183], v221 offset:224\n\t"
        "v_exp_f32 v116, v116\n\t"
        "v_exp_f32 v117, v117\n\t"
        "v_exp_f32 v118, v118\n\t"
        "v_exp_f32 v119, v119\n\t"
        "v_mfma_f32_32x32x16_bf16 v[132:147], v[172:175], v[20:23], v[132:147]\n\t"
        "ds_read_b64_tr_b16 v[184:185], v221 offset:896\n\t"
        "v_exp_f32 v120, v120\n\t"
        "v_exp_f32 v121, v121\n\t"
        "v_exp_f32 v122, v122\n\t"
        "v_exp_f32 v123, v123\n\t"
        "v_smfmac_f32_32x32x32_bf16 v[36:51], v[192:195], v[108:115], v223\n\t"
        "ds_read_b64_tr_b16 v[186:187], v221 offset:1120\n\t"
        "v_exp_f32 v124, v124\n\t"
        "v_exp_f32 v125, v125\n\t"
        "v_exp_f32 v126, v126\n\t"
        "v_exp_f32 v127, v127\n\t"
        "v_smfmac_f32_32x32x32_bf16 v[52:67], v[188:191], v[116:123], v223\n\t"
        "v_exp_f32 v128, v128\n\t"
        "v_exp_f32 v129, v129\n\t"
        "v_exp_f32 v130, v130\n\t"
        "v_exp_f32 v131, v131\n\t"
        "v_mfma_f32_32x32x16_bf16 v[148:163], v[176:179], v[32:35], 0\n\t"
        "s_add_u32 s50, s50, s47\n\t"
        "s_addc_u32 s51, s51, 0\n\t"
        "v_exp_f32 v132, v132\n\t"
        "v_exp_f32 v133, v133\n\t"
        "v_exp_f32 v134, v134\n\t"
        "v_exp_f32 v135, v135\n\t"
        "v_xor_b32 v222, 0x4000, v222\n\t"
        "v_mfma_f32_32x32x16_bf16 v[148:163], v[172:175], v[28:31], v[148:163]\n\t"
        "s_add_u32 s52, s52, s47\n\t"
        "s_addc_u32 s53, s53, 0\n\t"
        "v_exp_f32 v136, v136\n\t"
        "v_exp_f32 v137, v137\n\t"
        "v_exp_f32 v138, v138\n\t"
        "v_exp_f32 v139, v139\n\t"
        "v_smfmac_f32_32x32x32_bf16 v[52:67], v[192:195], v[124:131], v223\n\t"
        "s_add_u32 s46, s46, 1\n\t"
        "s_and_b32 s46, s46, 3\n\t"
        "v_exp_f32 v140, v140\n\t"
        "v_exp_f32 v141, v141\n\t"
        "v_exp_f32 v142, v142\n\t"
        "v_exp_f32 v143, v143\n\t"
        "v_smfmac_f32_32x32x32_bf16 v[68:83], v[188:191], v[132:139], v223\n\t"
        "s_waitcnt lgkmcnt(0)\n\t"
        "v_exp_f32 v144, v144\n\t"
        "v_exp_f32 v145, v145\n\t"
        "v_exp_f32 v146, v146\n\t"
        "v_exp_f32 v147, v147\n\t"
        "v_mfma_f32_32x32x16_bf16 v[100:115], v[168:171], v[8:11], 0\n\t"
        "s_add_u32 s45, s45, s47\n\t"
        "s_cmp_eq_u32 s46, 3\n\t"
        "v_exp_f32 v148, v148\n\t"
        "v_exp_f32 v149, v149\n\t"
        "v_exp_f32 v150, v150\n\t"
        "v_exp_f32 v151, v151\n\t"
        "v_mfma_f32_32x32x16_bf16 v[100:115], v[164:167], v[4:7], v[100:115]\n\t"
        "s_cselect_b32 s45, s63, s45\n\t"
        "s_add_u32 s44, s44, 1\n\t"
        "v_exp_f32 v152, v152\n\t"
        "v_exp_f32 v153, v153\n\t"
        "v_exp_f32 v154, v154\n\t"
        "v_exp_f32 v155, v155\n\t"
        "v_smfmac_f32_32x32x32_bf16 v[68:83], v[192:195], v[140:147], v223\n\t"
        "v_exp_f32 v156, v156\n\t"
        "v_exp_f32 v157, v157\n\t"
        "v_exp_f32 v158, v158\n\t"
        "v_exp_f32 v159, v159\n\t"
        "v_smfmac_f32_32x32x32_bf16 v[84:99], v[188:191], v[148:155], v223\n\t"
        "s_cmp_lt_u32 s44, 7\n\t"
        "v_exp_f32 v160, v160\n\t"
        "v_exp_f32 v161, v161\n\t"
        "v_exp_f32 v162, v162\n\t"
        "v_exp_f32 v163, v163\n\t"
        "s_cbranch_scc1 1b\n\t"
        "s_branch 11f\n\t"
        ".p2align 12\n\t"
        "11:\n\t"
        "v_mfma_f32_32x32x16_bf16 v[116:131], v[168:171], v[16:19], 0\n\t"
        "ds_read_b128 v[176:179], v220 offset:11264\n\t"
        "v_exp_f32 v100, v100\n\t"
        "v_exp_f32 v101, v101\n\t"
        "v_exp_f32 v102, v102\n\t"
        "v_exp_f32 v103, v103\n\t"
        "v_mfma_f32_32x32x16_bf16 v[116:131], v[164:167], v[12:15], v[116:131]\n\t"
        "ds_read_b128 v[172:175], v220 offset:10240\n\t"
        "v_exp_f32 v104, v104\n\t"
        "v_exp_f32 v105, v105\n\t"
        "v_exp_f32 v106, v106\n\t"
        "v_exp_f32 v107, v107\n\t"
        "v_smfmac_f32_32x32x32_bf16 v[84:99], v[192:195], v[156:163], v223\n\t"
        "ds_read_b64_tr_b16 v[188:189], v221 offset:1792\n\t"
        "v_exp_f32 v108, v108\n\t"
        "v_exp_f32 v109, v109\n\t"
        "v_exp_f32 v110, v110\n\t"
        "v_exp_f32 v111, v111\n\t"
        "v_smfmac_f32_32x32x32_bf16 v[36:51], v[180:183], v[100:107], v223\n\t"
        "s_waitcnt lgkmcnt(2)\n\t"
        "ds_read_b64_tr_b16 v[190:191], v221 offset:2016\n\t"
        "v_exp_f32 v112, v112\n\t"
        "v_exp_f32 v113, v113\n\t"
        "v_exp_f32 v114, v114\n\t"
        "v_exp_f32 v115, v115\n\t"
        "v_mfma_f32_32x32x16_bf16 v[132:147], v[176:179], v[8:11], 0\n\t"
        "s_waitcnt lgkmcnt(2)\n\t"
        "ds_read_b64_tr_b16 v[192:193], v221 offset:2688\n\t"
        "v_exp_f32 v116, v116\n\t"
        "v_exp_f32 v117, v117\n\t"
        "v_exp_f32 v118, v118\n\t"
        "v_exp_f32 v119, v119\n\t"
        "v_mfma_f32_32x32x16_bf16 v[132:147], v[172:175], v[4:7], v[132:147]\n\t"
        "ds_read_b64_tr_b16 v[194:195], v221 offset:2912\n\t"
        "v_exp_f32 v120, v120\n\t"
        "v_exp_f32 v121, v121\n\t"
        "v_exp_f32 v122, v122\n\t"
        "v_exp_f32 v123, v123\n\t"
        "v_smfmac_f32_32x32x32_bf16 v[36:51], v[184:187], v[108:115], v223\n\t"
        "ds_read_b128 v[200:203], v220 offset:13312\n\t"
        "v_exp_f32 v124, v124\n\t"
        "v_exp_f32 v125, v125\n\t"
        "v_exp_f32 v126, v126\n\t"
        "v_exp_f32 v127, v127\n\t"
        "v_smfmac_f32_32x32x32_bf16 v[52:67], v[180:183], v[116:123], v223\n\t"
        "ds_read_b128 v[196:199], v220 offset:12288\n\t"
        "v_exp_f32 v128, v128\n\t"
        "v_exp_f32 v129, v129\n\t"
        "v_exp_f32 v130, v130\n\t"
        "v_exp_f32 v131, v131\n\t"
        "v_mfma_f32_32x32x16_bf16 v[148:163], v[176:179], v[16:19], 0\n\t"
        "ds_read_b64_tr_b16 v[204:205], v221 offset:3584\n\t"
        "v_exp_f32 v132, v132\n\t"
        "v_exp_f32 v133, v133\n\t"
        "v_exp_f32 v134, v134\n\t"
        "v_exp_f32 v135, v135\n\t"
        "v_mfma_f32_32x32x16_bf16 v[148:163], v[172:175], v[12:15], v[148:163]\n\t"
        "ds_read_b64_tr_b16 v[206:207], v221 offset:3808\n\t"
        "v_exp_f32 v136, v136\n\t"
        "v_exp_f32 v137, v137\n\t"
        "v_exp_f32 v138, v138\n\t"
        "v_exp_f32 v139, v139\n\t"
        "v_smfmac_f32_32x32x32_bf16 v[52:67], v[184:187], v[124:131], v223\n\t"
        "s_waitcnt lgkmcnt(6)\n\t"
        "ds_read_b64_tr_b16 v[208:209], v221 offset:4480\n\t"
        "v_exp_f32 v140, v140\n\t"
        "v_exp_f32 v141, v141\n\t"
        "v_exp_f32 v142, v142\n\t"
        "v_exp_f32 v143, v143\n\t"
        "v_smfmac_f32_32x32x32_bf16 v[36:51], v[188:191], v[132:139], v223\n\t"
        "s_waitcnt lgkmcnt(4)\n\t"
        "ds_read_b64_tr_b16 v[210:211], v221 offset:4704\n\t"
        "v_exp_f32 v144, v144\n\t"
        "v_exp_f32 v145, v145\n\t"
        "v_exp_f32 v146, v146\n\t"
        "v_exp_f32 v147, v147\n\t"
        "v_mfma_f32_32x32x16_bf16 v[100:115], v[200:203], v[8:11], 0\n\t"
        "s_waitcnt lgkmcnt(4)\n\t"
        "ds_read_b128 v[236:239], v220 offset:15360\n\t"
        "v_exp_f32 v148, v148\n\t"
        "v_exp_f32 v149, v149\n\t"
        "v_exp_f32 v150, v150\n\t"
        "v_exp_f32 v151, v151\n\t"
        "v_mfma_f32_32x32x16_bf16 v[100:115], v[196:199], v[4:7], v[100:115]\n\t"
        "ds_read_b128 v[232:235], v220 offset:14336\n\t"
        "v_exp_f32 v152, v152\n\t"
        "v_exp_f32 v153, v153\n\t"
        "v_exp_f32 v154, v154\n\t"
        "v_exp_f32 v155, v155\n\t"
        "v_smfmac_f32_32x32x32_bf16 v[36:51], v[192:195], v[140:147], v223\n\t"
        "ds_read_b64_tr_b16 v[240:241], v221 offset:5376\n\t"
        "v_exp_f32 v156, v156\n\t"
        "v_exp_f32 v157, v157\n\t"
        "v_exp_f32 v158, v158\n\t"
        "v_exp_f32 v159, v159\n\t"
        "v_smfmac_f32_32x32x32_bf16 v[52:67], v[188:191], v[148:155], v223\n\t"
        "ds_read_b64_tr_b16 v[242:243], v221 offset:5600\n\t"
        "v_exp_f32 v160, v160\n\t"
        "v_exp_f32 v161, v161\n\t"
        "v_exp_f32 v162, v162\n\t"
        "v_exp_f32 v163, v163\n\t"
        "v_mfma_f32_32x32x16_bf16 v[116:131], v[200:203], v[16:19], 0\n\t"
        "ds_read_b64_tr_b16 v[244:245], v221 offset:6272\n\t"
        "v_exp_f32 v100, v100\n\t"
        "v_exp_f32 v101, v101\n\t"
        "v_exp_f32 v102, v102\n\t"
        "v_exp_f32 v103, v103\n\t"
        "v_mfma_f32_32x32x16_bf16 v[116:131], v[196:199], v[12:15], v[116:131]\n\t"
        "ds_read_b64_tr_b16 v[246:247], v221 offset:6496\n\t"
        "v_exp_f32 v104, v104\n\t"
        "v_exp_f32 v105, v105\n\t"
        "v_exp_f32 v106, v106\n\t"
        "v_exp_f32 v107, v107\n\t"
        "v_smfmac_f32_32x32x32_bf16 v[52:67], v[192:195], v[156:163], v223\n\t"
        "s_waitcnt lgkmcnt(8)\n\t"
        "v_exp_f32 v108, v108\n\t"
        "v_exp_f32 v109, v109\n\t"
        "v_exp_f32 v110, v110\n\t"
        "v_exp_f32 v111, v111\n\t"
        "v_smfmac_f32_32x32x32_bf16 v[36:51], v[204:207], v[100:107], v223\n\t"
        "s_waitcnt lgkmcnt(5)\n\t"
        "v_exp_f32 v112, v112\n\t"
        "v_exp_f32 v113, v113\n\t"
        "v_exp_f32 v114, v114\n\t"
        "v_exp_f32 v115, v115\n\t"
        "v_mfma_f32_32x32x16_bf16 v[132:147], v[236:239], v[8:11], 0\n\t"
        "s_waitcnt lgkmcnt(4)\n\t"
        "v_exp_f32 v116, v116\n\t"
        "v_exp_f32 v117, v117\n\t"
        "v_exp_f32 v118, v118\n\t"
        "v_exp_f32 v119, v119\n\t"
        "v_mfma_f32_32x32x16_bf16 v[132:147], v[232:235], v[4:7], v[132:147]\n\t"
        "v_exp_f32 v120, v120\n\t"
        "v_exp_f32 v121, v121\n\t"
        "v_exp_f32 v122, v122\n\t"
        "v_exp_f32 v123, v123\n\t"
        "v_smfmac_f32_32x32x32_bf16 v[36:51], v[208:211], v[108:115], v223\n\t"
        "v_exp_f32 v124, v124\n\t"
        "v_exp_f32 v125, v125\n\t"
        "v_exp_f32 v126, v126\n\t"
        "v_exp_f32 v127, v127\n\t"
        "v_smfmac_f32_32x32x32_bf16 v[52:67], v[204:207], v[116:123], v223\n\t"
        "v_exp_f32 v128, v128\n\t"
        "v_exp_f32 v129, v129\n\t"
        "v_exp_f32 v130, v130\n\t"
        "v_exp_f32 v131, v131\n\t"
        "v_mfma_f32_32x32x16_bf16 v[148:163], v[236:239], v[16:19], 0\n\t"
        "v_exp_f32 v132, v132\n\t"
        "v_exp_f32 v133, v133\n\t"
        "v_exp_f32 v134, v134\n\t"
        "v_exp_f32 v135, v135\n\t"
        "v_mfma_f32_32x32x16_bf16 v[148:163], v[232:235], v[12:15], v[148:163]\n\t"
        "v_exp_f32 v136, v136\n\t"
        "v_exp_f32 v137, v137\n\t"
        "v_exp_f32 v138, v138\n\t"
        "v_exp_f32 v139, v139\n\t"
        "v_smfmac_f32_32x32x32_bf16 v[52:67], v[208:211], v[124:131], v223\n\t"
        "s_waitcnt lgkmcnt(2)\n\t"
        "v_exp_f32 v140, v140\n\t"
        "v_exp_f32 v141, v141\n\t"
        "v_exp_f32 v142, v142\n\t"
        "v_exp_f32 v143, v143\n\t"
        "v_smfmac_f32_32x32x32_bf16 v[36:51], v[240:243], v[132:139], v223\n\t"
        "v_exp_f32 v144, v144\n\t"
        "v_exp_f32 v145, v145\n\t"
        "v_exp_f32 v146, v146\n\t"
        "v_exp_f32 v147, v147\n\t"
        "v_mfma_f32_32x32x16_bf16 v[100:115], v[168:171], v[24:27], 0\n\t"
        "v_exp_f32 v148, v148\n\t"
        "v_exp_f32 v149, v149\n\t"
        "v_exp_f32 v150, v150\n\t"
        "v_exp_f32 v151, v151\n\t"
        "v_mfma_f32_32x32x16_bf16 v[100:115], v[164:167], v[20:23], v[100:115]\n\t"
        "s_waitcnt lgkmcnt(0)\n\t"
        "v_exp_f32 v152, v152\n\t"
        "v_exp_f32 v153, v153\n\t"
        "v_exp_f32 v154, v154\n\t"
        "v_exp_f32 v155, v155\n\t"
        "v_smfmac_f32_32x32x32_bf16 v[36:51], v[244:247], v[140:147], v223\n\t"
        "v_exp_f32 v156, v156\n\t"
        "v_exp_f32 v157, v157\n\t"
        "v_exp_f32 v158, v158\n\t"
        "v_exp_f32 v159, v159\n\t"
        "v_smfmac_f32_32x32x32_bf16 v[52:67], v[240:243], v[148:155], v223\n\t"
        "v_exp_f32 v160, v160\n\t"
        "v_exp_f32 v161, v161\n\t"
        "v_exp_f32 v162, v162\n\t"
        "v_exp_f32 v163, v163\n\t"
        "v_mfma_f32_32x32x16_bf16 v[116:131], v[176:179], v[24:27], 0\n\t"
        "v_exp_f32 v100, v100\n\t"
        "v_exp_f32 v101, v101\n\t"
        "v_exp_f32 v102, v102\n\t"
        "v_exp_f32 v103, v103\n\t"
        "v_mfma_f32_32x32x16_bf16 v[116:131], v[172:175], v[20:23], v[116:131]\n\t"
        "ds_write_b128 v228, v[36:39] offset:24576\n\t"
        "v_exp_f32 v104, v104\n\t"
        "v_exp_f32 v105, v105\n\t"
        "v_exp_f32 v106, v106\n\t"
        "v_exp_f32 v107, v107\n\t"
        "v_smfmac_f32_32x32x32_bf16 v[52:67], v[244:247], v[156:163], v223\n\t"
        "ds_write_b128 v228, v[40:43] offset:25600\n\t"
        "s_setprio 1\n\t"
        "v_exp_f32 v108, v108\n\t"
        "v_exp_f32 v109, v109\n\t"
        "v_exp_f32 v110, v110\n\t"
        "v_exp_f32 v111, v111\n\t"
        "v_smfmac_f32_32x32x32_bf16 v[68:83], v[180:183], v[100:107], v223\n\t"
        "ds_write_b128 v228, v[44:47] offset:26624\n\t"
        "v_exp_f32 v112, v112\n\t"
        "v_exp_f32 v113, v113\n\t"
        "v_exp_f32 v114, v114\n\t"
        "v_exp_f32 v115, v115\n\t"
        "v_mfma_f32_32x32x16_bf16 v[132:147], v[200:203], v[24:27], 0\n\t"
        "ds_write_b128 v228, v[48:51] offset:27648\n\t"
        "v_exp_f32 v116, v116\n\t"
        "v_exp_f32 v117, v117\n\t"
        "v_exp_f32 v118, v118\n\t"
        "v_exp_f32 v119, v119\n\t"
        "v_mfma_f32_32x32x16_bf16 v[132:147], v[196:199], v[20:23], v[132:147]\n\t"
        "ds_write_b128 v228, v[52:55] offset:28672\n\t"
        "v_exp_f32 v120, v120\n\t"
        "v_exp_f32 v121, v121\n\t"
        "v_exp_f32 v122, v122\n\t"
        "v_exp_f32 v123, v123\n\t"
        "v_smfmac_f32_32x32x32_bf16 v[68:83], v[184:187], v[108:115], v223\n\t"
        "ds_write_b128 v228, v[56:59] offset:29696\n\t"
        "v_exp_f32 v124, v124\n\t"
        "v_exp_f32 v125, v125\n\t"
        "v_exp_f32 v126, v126\n\t"
        "v_exp_f32 v127, v127\n\t"
        "v_smfmac_f32_32x32x32_bf16 v[68:83], v[188:191], v[116:123], v223\n\t"
        "ds_write_b128 v228, v[60:63] offset:30720\n\t"
        "v_exp_f32 v128, v128\n\t"
        "v_exp_f32 v129, v129\n\t"
        "v_exp_f32 v130, v130\n\t"
        "v_exp_f32 v131, v131\n\t"
        "v_mfma_f32_32x32x16_bf16 v[148:163], v[236:239], v[24:27], 0\n\t"
        "ds_write_b128 v228, v[64:67] offset:31744\n\t"
        "s_setprio 0\n\t"
        "v_exp_f32 v132, v132\n\t"
        "v_exp_f32 v133, v133\n\t"
        "v_exp_f32 v134, v134\n\t"
        "v_exp_f32 v135, v135\n\t"
        "v_mfma_f32_32x32x16_bf16 v[148:163], v[232:235], v[20:23], v[148:163]\n\t"
        "v_exp_f32 v136, v136\n\t"
        "v_exp_f32 v137, v137\n\t"
        "v_exp_f32 v138, v138\n\t"
        "v_exp_f32 v139, v139\n\t"
        "v_smfmac_f32_32x32x32_bf16 v[68:83], v[192:195], v[124:131], v223\n\t"
        "s_waitcnt lgkmcnt(0)\n\t"
        "s_barrier\n\t"
        "ds_read_b128 v[52:55], v229 offset:28672\n\t"
        "ds_read_b128 v[56:59], v229 offset:29696\n\t"
        "v_exp_f32 v140, v140\n\t"
        "v_exp_f32 v141, v141\n\t"
        "v_exp_f32 v142, v142\n\t"
        "v_exp_f32 v143, v143\n\t"
        "v_smfmac_f32_32x32x32_bf16 v[68:83], v[204:207], v[132:139], v223\n\t"
        "ds_read_b128 v[60:63], v229 offset:30720\n\t"
        "ds_read_b128 v[64:67], v229 offset:31744\n\t"
        "s_setprio 2\n\t"
        "v_exp_f32 v144, v144\n\t"
        "v_exp_f32 v145, v145\n\t"
        "v_exp_f32 v146, v146\n\t"
        "v_exp_f32 v147, v147\n\t"
        "v_mfma_f32_32x32x16_bf16 v[100:115], v[168:171], v[32:35], 0\n\t"
        "ds_read_b128 v[36:39], v229 offset:24576\n\t"
        "ds_read_b128 v[40:43], v229 offset:25600\n\t"
        "v_exp_f32 v148, v148\n\t"
        "v_exp_f32 v149, v149\n\t"
        "v_exp_f32 v150, v150\n\t"
        "v_exp_f32 v151, v151\n\t"
        "v_mfma_f32_32x32x16_bf16 v[100:115], v[164:167], v[28:31], v[100:115]\n\t"
        "ds_read_b128 v[44:47], v229 offset:26624\n\t"
        "ds_read_b128 v[48:51], v229 offset:27648\n\t"
        "v_exp_f32 v152, v152\n\t"
        "v_exp_f32 v153, v153\n\t"
        "v_exp_f32 v154, v154\n\t"
        "v_exp_f32 v155, v155\n\t"
        "v_smfmac_f32_32x32x32_bf16 v[68:83], v[208:211], v[140:147], v223\n\t"
        "s_setprio 3\n\t"
        "v_exp_f32 v156, v156\n\t"
        "v_exp_f32 v157, v157\n\t"
        "v_exp_f32 v158, v158\n\t"
        "v_exp_f32 v159, v159\n\t"
        "s_waitcnt lgkmcnt(7)\n\t"
        "v_pk_add_f32 v[84:85], v[84:85], v[52:53]\n\t"
        "v_pk_add_f32 v[86:87], v[86:87], v[54:55]\n\t"
        "s_waitcnt lgkmcnt(6)\n\t"
        "v_pk_add_f32 v[88:89], v[88:89], v[56:57]\n\t"
        "v_pk_add_f32 v[90:91], v[90:91], v[58:59]\n\t"
        "v_smfmac_f32_32x32x32_bf16 v[68:83], v[240:243], v[148:155], v223\n\t"
        "v_exp_f32 v160, v160\n\t"
        "v_exp_f32 v161, v161\n\t"
        "v_exp_f32 v162, v162\n\t"
        "v_exp_f32 v163, v163\n\t"
        "s_waitcnt lgkmcnt(5)\n\t"
        "v_pk_add_f32 v[92:93], v[92:93], v[60:61]\n\t"
        "v_pk_add_f32 v[94:95], v[94:95], v[62:63]\n\t"
        "s_waitcnt lgkmcnt(4)\n\t"
        "v_pk_add_f32 v[96:97], v[96:97], v[64:65]\n\t"
        "v_pk_add_f32 v[98:99], v[98:99], v[66:67]\n\t"
        "v_mfma_f32_32x32x16_bf16 v[116:131], v[176:179], v[32:35], 0\n\t"
        "s_setprio 2\n\t"
        "v_exp_f32 v100, v100\n\t"
        "v_exp_f32 v101, v101\n\t"
        "v_exp_f32 v102, v102\n\t"
        "v_exp_f32 v103, v103\n\t"
        "v_mfma_f32_32x32x16_bf16 v[116:131], v[172:175], v[28:31], v[116:131]\n\t"
        "v_exp_f32 v104, v104\n\t"
        "v_exp_f32 v105, v105\n\t"
        "v_exp_f32 v106, v106\n\t"
        "v_exp_f32 v107, v107\n\t"
        "v_smfmac_f32_32x32x32_bf16 v[68:83], v[244:247], v[156:163], v223\n\t"
        "v_exp_f32 v108, v108\n\t"
        "v_exp_f32 v109, v109\n\t"
        "v_exp_f32 v110, v110\n\t"
        "v_exp_f32 v111, v111\n\t"
        "v_smfmac_f32_32x32x32_bf16 v[84:99], v[180:183], v[100:107], v223\n\t"
        "v_exp_f32 v112, v112\n\t"
        "v_exp_f32 v113, v113\n\t"
        "v_exp_f32 v114, v114\n\t"
        "v_exp_f32 v115, v115\n\t"
        "v_mfma_f32_32x32x16_bf16 v[132:147], v[200:203], v[32:35], 0\n\t"
        "v_exp_f32 v116, v116\n\t"
        "v_exp_f32 v117, v117\n\t"
        "v_exp_f32 v118, v118\n\t"
        "v_exp_f32 v119, v119\n\t"
        "s_waitcnt lgkmcnt(0)\n\t"
        "v_add_f32 v80, v80, v48\n\t"
        "v_mov_b32 v2, v80\n\t"
        "v_add_f32 v81, v81, v49\n\t"
        "v_mfma_f32_32x32x16_bf16 v[132:147], v[196:199], v[28:31], v[132:147]\n\t"
        "v_exp_f32 v120, v120\n\t"
        "v_exp_f32 v121, v121\n\t"
        "v_exp_f32 v122, v122\n\t"
        "v_exp_f32 v123, v123\n\t"
        "v_permlane32_swap_b32 v80, v2\n\t"
        "v_rcp_f32 v2, v2\n\t"
        "v_smfmac_f32_32x32x32_bf16 v[84:99], v[184:187], v[108:115], v223\n\t"
        "s_setprio 3\n\t"
        "v_exp_f32 v124, v124\n\t"
        "v_exp_f32 v125, v125\n\t"
        "v_exp_f32 v126, v126\n\t"
        "v_exp_f32 v127, v127\n\t"
        "v_pk_add_f32 v[82:83], v[82:83], v[50:51]\n\t"
        "v_pk_add_f32 v[68:69], v[68:69], v[36:37]\n\t"
        "v_pk_add_f32 v[70:71], v[70:71], v[38:39]\n\t"
        "v_pk_add_f32 v[72:73], v[72:73], v[40:41]\n\t"
        "v_pk_add_f32 v[74:75], v[74:75], v[42:43]\n\t"
        "v_pk_add_f32 v[76:77], v[76:77], v[44:45]\n\t"
        "v_pk_add_f32 v[78:79], v[78:79], v[46:47]\n\t"
        "v_smfmac_f32_32x32x32_bf16 v[84:99], v[188:191], v[116:123], v223\n\t"
        "v_exp_f32 v128, v128\n\t"
        "v_exp_f32 v129, v129\n\t"
        "v_exp_f32 v130, v130\n\t"
        "v_exp_f32 v131, v131\n\t"
        "v_pk_mul_f32 v[80:81], v[2:3], v[80:81] op_sel_hi:[0,1]\n\t"
        "v_pk_mul_f32 v[82:83], v[2:3], v[82:83] op_sel_hi:[0,1]\n\t"
        "v_pk_mul_f32 v[68:69], v[2:3], v[68:69] op_sel_hi:[0,1]\n\t"
        "v_pk_mul_f32 v[70:71], v[2:3], v[70:71] op_sel_hi:[0,1]\n\t"
        "v_pk_mul_f32 v[72:73], v[2:3], v[72:73] op_sel_hi:[0,1]\n\t"
        "v_pk_mul_f32 v[74:75], v[2:3], v[74:75] op_sel_hi:[0,1]\n\t"
        "v_pk_mul_f32 v[76:77], v[2:3], v[76:77] op_sel_hi:[0,1]\n\t"
        "v_pk_mul_f32 v[78:79], v[2:3], v[78:79] op_sel_hi:[0,1]\n\t"
        "v_mfma_f32_32x32x16_bf16 v[148:163], v[236:239], v[32:35], 0\n\t"
        "s_setprio 2\n\t"
        "v_exp_f32 v132, v132\n\t"
        "v_exp_f32 v133, v133\n\t"
        "v_exp_f32 v134, v134\n\t"
        "v_exp_f32 v135, v135\n\t"
        "v_cvt_pk_bf16_f32 v42, v80, v81\n\t"
        "v_cvt_pk_bf16_f32 v43, v82, v83\n\t"
        "v_cvt_pk_bf16_f32 v36, v68, v69\n\t"
        "v_cvt_pk_bf16_f32 v37, v70, v71\n\t"
        "v_mfma_f32_32x32x16_bf16 v[148:163], v[232:235], v[28:31], v[148:163]\n\t"
        "ds_write_b64 v230, v[42:43] offset:24624\n\t"
        "ds_write_b64 v230, v[36:37] offset:24576\n\t"
        "v_exp_f32 v136, v136\n\t"
        "v_exp_f32 v137, v137\n\t"
        "v_exp_f32 v138, v138\n\t"
        "v_exp_f32 v139, v139\n\t"
        "v_cvt_pk_bf16_f32 v38, v72, v73\n\t"
        "v_cvt_pk_bf16_f32 v39, v74, v75\n\t"
        "v_cvt_pk_bf16_f32 v40, v76, v77\n\t"
        "v_cvt_pk_bf16_f32 v41, v78, v79\n\t"
        "v_smfmac_f32_32x32x32_bf16 v[84:99], v[192:195], v[124:131], v223\n\t"
        "ds_write_b64 v230, v[38:39] offset:24592\n\t"
        "ds_write_b64 v230, v[40:41] offset:24608\n\t"
        "ds_read_b128 v[52:55], v229 offset:24576\n\t"
        "ds_read_b128 v[56:59], v229 offset:25600\n\t"
        "s_setprio 1\n\t"
        "v_exp_f32 v140, v140\n\t"
        "v_exp_f32 v141, v141\n\t"
        "v_exp_f32 v142, v142\n\t"
        "v_exp_f32 v143, v143\n\t"
        "v_smfmac_f32_32x32x32_bf16 v[84:99], v[204:207], v[132:139], v223\n\t"
        "v_exp_f32 v144, v144\n\t"
        "v_exp_f32 v145, v145\n\t"
        "v_exp_f32 v146, v146\n\t"
        "v_exp_f32 v147, v147\n\t"
        "s_waitcnt lgkmcnt(1)\n\t"
        "global_store_dwordx4 v231, v[52:55], s[56:57] offset:0 sc1\n\t"
        "s_mov_b32 exec_hi, 0xffff\n\t"
        "s_waitcnt lgkmcnt(0)\n\t"
        "global_store_dwordx4 v231, v[56:59], s[56:57] offset:1024 sc1\n\t"
        "s_mov_b32 exec_hi, -1\n\t"
        "v_exp_f32 v148, v148\n\t"
        "v_exp_f32 v149, v149\n\t"
        "v_exp_f32 v150, v150\n\t"
        "v_exp_f32 v151, v151\n\t"
        "v_exp_f32 v152, v152\n\t"
        "v_exp_f32 v153, v153\n\t"
        "v_exp_f32 v154, v154\n\t"
        "v_exp_f32 v155, v155\n\t"
        "v_smfmac_f32_32x32x32_bf16 v[84:99], v[208:211], v[140:147], v223\n\t"
        "v_exp_f32 v156, v156\n\t"
        "v_exp_f32 v157, v157\n\t"
        "v_exp_f32 v158, v158\n\t"
        "v_exp_f32 v159, v159\n\t"
        "v_smfmac_f32_32x32x32_bf16 v[84:99], v[240:243], v[148:155], v223\n\t"
        "s_setprio 0\n\t"
        "v_exp_f32 v160, v160\n\t"
        "v_exp_f32 v161, v161\n\t"
        "v_exp_f32 v162, v162\n\t"
        "v_exp_f32 v163, v163\n\t"
        "s_nop 1\n\t"
        "v_smfmac_f32_32x32x32_bf16 v[84:99], v[244:247], v[156:163], v223\n\t"
        "s_mov_b32 %[ok], 1\n\t"
        "s_nop 10\n\t"
        "v_mov_b32 v2, v96\n\t"
        "s_nop 1\n\t"
        "v_permlane32_swap_b32 v96, v2\n\t"
        "v_rcp_f32 v2, v2\n\t"
        "s_nop 0\n\t"
        "v_pk_mul_f32 v[96:97], v[2:3], v[96:97] op_sel_hi:[0,1]\n\t"
        "v_pk_mul_f32 v[98:99], v[2:3], v[98:99] op_sel_hi:[0,1]\n\t"
        "v_pk_mul_f32 v[84:85], v[2:3], v[84:85] op_sel_hi:[0,1]\n\t"
        "v_pk_mul_f32 v[86:87], v[2:3], v[86:87] op_sel_hi:[0,1]\n\t"
        "v_cvt_pk_bf16_f32 v50, v96, v97\n\t"
        "v_cvt_pk_bf16_f32 v51, v98, v99\n\t"
        "v_pk_mul_f32 v[88:89], v[2:3], v[88:89] op_sel_hi:[0,1]\n\t"
        "ds_write_b64 v230, v[50:51] offset:26672\n\t"
        "v_pk_mul_f32 v[90:91], v[2:3], v[90:91] op_sel_hi:[0,1]\n\t"
        "v_cvt_pk_bf16_f32 v44, v84, v85\n\t"
        "v_cvt_pk_bf16_f32 v45, v86, v87\n\t"
        "v_pk_mul_f32 v[92:93], v[2:3], v[92:93] op_sel_hi:[0,1]\n\t"
        "ds_write_b64 v230, v[44:45] offset:26624\n\t"
        "v_pk_mul_f32 v[94:95], v[2:3], v[94:95] op_sel_hi:[0,1]\n\t"
        "v_cvt_pk_bf16_f32 v46, v88, v89\n\t"
        "v_cvt_pk_bf16_f32 v47, v90, v91\n\t"
        "ds_write_b64 v230, v[46:47] offset:26640\n\t"
        "v_cvt_pk_bf16_f32 v48, v92, v93\n\t"
        "v_cvt_pk_bf16_f32 v49, v94, v95\n\t"
        "ds_write_b64 v230, v[48:49] offset:26656\n\t"
        "ds_read_b128 v[60:63], v229 offset:26624\n\t"
        "ds_read_b128 v[64:67], v229 offset:27648\n\t"
        "s_waitcnt lgkmcnt(1)\n\t"
        "global_store_dwordx4 v231, v[60:63], s[56:57] offset:1792 sc1\n\t"
        "s_mov_b32 exec_hi, 0xffff\n\t"
        "s_waitcnt lgkmcnt(0)\n\t"
        "global_store_dwordx4 v231, v[64:67], s[56:57] offset:2816 sc1\n\t"
        "s_mov_b32 exec_hi, -1\n\t"
        "s_branch 8f\n\t"
        "9:\n\t"
        "s_setprio 0\n\t"
        "s_waitcnt vmcnt(0) lgkmcnt(0)\n\t"
        "s_barrier\n\t"
        "8:\n\t"
        : [ok] "=&s"(done), [tid] "+v"(tid)
        : [ka] "s"((uint64_t)(uintptr_t)__builtin_amdgcn_kernarg_segment_ptr()), [bx] "s"((int)blockIdx.x)
        : "memory", "scc", "vcc", "v2", "v3", "v4", "v5", "v6", "v7", "v8", "v9", "v10", "v11", "v12", "v13", "v14", "v15", "v16", "v17", "v18", "v19", "v20", "v21", "v22", "v23", "v24", "v25", "v26", "v27", "v28", "v29", "v30", "v31", "v32", "v33", "v34", "v35", "v36", "v37", "v38", "v39", "v40", "v41", "v42", "v43", "v44", "v45", "v46", "v47", "v48", "v49", "v50", "v51", "v52", "v53", "v54", "v55", "v56", "v57", "v58", "v59", "v60", "v61", "v62", "v63", "v64", "v65", "v66", "v67", "v68", "v69", "v70", "v71", "v72", "v73", "v74", "v75", "v76", "v77", "v78", "v79", "v80", "v81", "v82", "v83", "v84", "v85", "v86", "v87", "v88", "v89", "v90", "v91", "v92", "v93", "v94", "v95", "v96", "v97", "v98", "v99", "v100", "v101", "v102", "v103", "v104", "v105", "v106", "v107", "v108", "v109", "v110", "v111", "v112", "v113", "v114", "v115", "v116", "v117", "v118", "v119", "v120", "v121", "v122", "v123", "v124", "v125", "v126", "v127", "v128", "v129", "v130", "v131", "v132", "v133", "v134", "v135", "v136", "v137", "v138", "v139", "v140", "v141", "v142", "v143", "v144", "v145", "v146", "v147", "v148", "v149", "v150", "v151", "v152", "v153", "v154", "v155", "v156", "v157", "v158", "v159", "v160", "v161", "v162", "v163", "v164", "v165", "v166", "v167", "v168", "v169", "v170", "v171", "v172", "v173", "v174", "v175", "v176", "v177", "v178", "v179", "v180", "v181", "v182", "v183", "v184", "v185", "v186", "v187", "v188", "v189", "v190", "v191", "v192", "v193", "v194", "v195", "v196", "v197", "v198", "v199", "v200", "v201", "v202", "v203", "v204", "v205", "v206", "v207", "v208", "v209", "v210", "v211", "v212", "v213", "v214", "v215", "v216", "v217", "v218", "v219", "v220", "v221", "v222", "v223", "v224", "v225", "v226", "v227", "v228", "v229", "v230", "v231", "v232", "v233", "v234", "v235", "v236", "v237", "v238", "v239", "v240", "v241", "v242", "v243", "v244", "v245", "v246", "v247", "v248", "v249", "v250", "v251", "v252", "v253", "v254", "v255", "s36", "s37", "s38", "s39", "s40", "s41", "s42", "s43", "s44", "s45", "s46", "s47", "s48", "s49", "s50", "s51", "s52", "s53", "s54", "s55", "s56", "s57", "s58", "s59", "s60", "s61", "s62", "s63", "s64", "s65", "s66", "s67", "s68", "s69", "s70", "s71");
    const int lane = tid & 63, wid = __builtin_amdgcn_readfirstlane(tid >> 6);
    if (!done) {
        const lds_ptr lds = (lds_ptr)smem;
        const unsigned lds0 = (unsigned)(uintptr_t)lds;
        const int hi = lane >> 5, l31 = lane & 31;
        const int bx = blockIdx.x, bh = (bx & 7) + 8 * (bx >> 5), quarter = (bx >> 3) & 3;
        const size_t headoff = (size_t)bh * SEQ * ROWB;
        const char* kbase = K + headoff + wid * 32 * ROWB;
        const char* qbase = Q + headoff + (quarter * 512 + wid * 64) * ROWB;
        const int voff = wid < 7 ? wid * 1792 : VSLOT - 2048;
        const char* vbase = V + headoff + voff;
        const unsigned vdst = lds0 + LDS_V + voff;
        const unsigned tw = T[(bh & (NHEADS - 1)) >> 1];
        const float tl = __uint_as_float((bh & 1) ? (tw & 0xffff0000u) : (tw << 16)) * LOG2E;
        *reinterpret_cast<LDS_AS u32x4*>(lds + LDS_ONES + tid * 16) = (u32x4){0x3f80u, 0u, 0x3f80u, 0u};
        const int sh = lane & 1;
        const int fwr = (((lane >> 1) ^ (sh << 2)) * 16 + sh * 512), frd = ((l31 ^ (hi << 2)) * 16 + hi * 512);
        const int vg = (lane >> 4) & 1, vq = (lane & 15) >> 2, vp = lane & 3;
        const bool ones_lane = (vg == 1 && vp == 3);
        const int vrd = ones_lane ? LDS_ONES + (hi ? 160 : 224) : LDS_V + (8 * hi + vq) * ROWB + 32 * vg + 8 * vp;
        const lds_ptr qt = lds + LDS_Q + wid * 4096;
        glds16x2(vbase + lane * 16, vdst);
        stage_tile(load_raw7(kbase + lane * 28), lds + KSLOT + wid * 2048 + fwr);
        stage_tile(load_raw7(qbase + lane * 28), qt + fwr);
        stage_tile(load_raw7(qbase + 32 * ROWB + lane * 28), qt + 2048 + fwr);
        WAIT_BAR();
        const f32x16 zero = {};
        f32x16 oa = zero, ob = zero;
        float ma = -INFINITY, mb = -INFINITY;
        const bf16x8 qa0 = lds_rd128(qt + frd), qa1 = lds_rd128(qt + 1024 + frd);
        const bf16x8 qb0 = lds_rd128(qt + 2048 + frd), qb1 = lds_rd128(qt + 3072 + frd);
        const int vlane = ones_lane ? 0 : 1;
        for (int c = 0; c < NCH; ++c) {
            if (c + 1 < NCH) {
                const Raw7 kr = load_raw7(kbase + (c + 1) * VSLOT + lane * 28);
                glds16x2(vbase + (c + 1) * VSLOT + lane * 16, vdst + ((c + 1) & 3) * VSLOT);
                stage_tile(kr, lds + (c & 1) * KSLOT + wid * 2048 + fwr);
            }
            const lds_ptr krc = lds + ((c & 1) ^ 1) * KSLOT + frd;
            for (int t = 0; t < TPC; ++t) {
                const lds_ptr vrc = lds + vrd + vlane * ((c & 3) * VSLOT + t * 1792);
                const bf16x8 k0 = lds_rd128(krc + t * 2048), k1 = lds_rd128(krc + t * 2048 + 1024);
                const bf16x8 v0 = vcat(lds_rdtr(vrc), lds_rdtr(vrc + 224));
                const bf16x8 v1 = vcat(lds_rdtr(vrc + 896), lds_rdtr(vrc + 896 + 224));
#define SAFE_TILE(q0_, q1_, m_, o_) do { \
                f32x16 s = QK(k0, q0_, zero); s = QK(k1, q1_, s); \
                float mx = -INFINITY; \
                _Pragma("unroll") for (int r = 0; r < 16; ++r) { s[r] *= tl; mx = __builtin_fmaxf(mx, s[r]); } \
                const float mn = __builtin_fmaxf(m_, xhalf_max(mx)); \
                const float al = __builtin_amdgcn_exp2f(m_ - mn); m_ = mn; \
                _Pragma("unroll") for (int r = 0; r < 16; ++r) { s[r] = __builtin_amdgcn_exp2f(s[r] - mn); o_[r] *= al; } \
                o_ = PV(v0, plo(s), o_); o_ = PV(v1, phi(s), o_); } while (0)
                SAFE_TILE(qa0, qa1, ma, oa);
                SAFE_TILE(qb0, qb1, mb, ob);
            }
            WAIT_BAR();
        }
        auto ra = __builtin_amdgcn_permlane32_swap(__float_as_uint(oa[12]), __float_as_uint(oa[12]), false, false);
        auto rb = __builtin_amdgcn_permlane32_swap(__float_as_uint(ob[12]), __float_as_uint(ob[12]), false, false);
        const float ia = __builtin_amdgcn_rcpf(__uint_as_float(ra[1])), ib = __builtin_amdgcn_rcpf(__uint_as_float(rb[1]));
        char* op = O + headoff + (quarter * 512 + wid * 64 + l31) * ROWB + 8 * hi;
#pragma unroll
        for (int g = 0; g < 4; ++g)
            if (g < 3 || hi == 0) {
                *reinterpret_cast<u32x2*>(op + 16 * g) =
                    (u32x2){pk_bf16((f32x2){oa[4 * g], oa[4 * g + 1]} * ia), pk_bf16((f32x2){oa[4 * g + 2], oa[4 * g + 3]} * ia)};
                *reinterpret_cast<u32x2*>(op + 32 * ROWB + 16 * g) =
                    (u32x2){pk_bf16((f32x2){ob[4 * g], ob[4 * g + 1]} * ib), pk_bf16((f32x2){ob[4 * g + 2], ob[4 * g + 3]} * ib)};
            }
    }
}

extern "C" void kernel_launch(void* const* d_in, const int* in_sizes, int n_in,
                              void* d_out, int out_size, void* d_ws, size_t ws_size,
                              hipStream_t stream) {
    (void)in_sizes; (void)n_in; (void)out_size; (void)d_ws; (void)ws_size;
    (void)hipFuncSetAttribute((const void*)qknorm_attn_kernel, hipFuncAttributeMaxDynamicSharedMemorySize, LDS_BYTES);
    hipLaunchKernelGGL(qknorm_attn_kernel, dim3(NBH * 4), dim3(512), LDS_BYTES, stream,
                       (const char*)d_in[0], (const char*)d_in[1], (const char*)d_in[2], (const unsigned*)d_in[3], (char*)d_out);
}
```
